```python
import torch

def _softmax(x, axis=-1):
    return torch.softmax(x, dim=axis)
def _gelu(x):
    return torch.nn.functional.gelu(x, approximate="tanh")
def _silu(x):
    return torch.nn.functional.silu(x)
def _sigmoid(x):
    return torch.sigmoid(x)
def _layernorm(x, axis=-1, eps=1e-5):
    m = x.mean(dim=axis, keepdim=True)
    v = x.var(dim=axis, keepdim=True, unbiased=False)
    return (x - m) / torch.sqrt(v + eps)
def _scatter_add(tgt, idx, src, axis=0):
    return tgt.clone().index_add_(axis, idx, src)
def _segment_sum(src, seg, n):
    out = torch.zeros((n,) + src.shape[1:], dtype=src.dtype)
    return out.index_add_(0, seg, src)
def _pad(x, pw):
    flat = tuple(v for pair in reversed(pw) for v in pair)
    return torch.nn.functional.pad(x, flat)

kernel_name = 'gated_mix__ssm_conv_h3__dense_activation_gelu__prompt'

def setup_inputs():

    _w = (lambda a: (lambda t: t.to(torch.bfloat16) if t.is_floating_point() else t)(torch.as_tensor(a)))
    def _A_setup():
        B, S, D = 1, 2048, 512
        ks = 4
        return {
            "q": _w((lambda *s: torch.randn(*s, dtype=torch.bfloat16) * 0.05)(B, S, D)),
            "k": _w((lambda *s: torch.randn(*s, dtype=torch.bfloat16) * 0.2)(B, S, D)),
            "v": _w((lambda *s: torch.randn(*s, dtype=torch.bfloat16) * 0.2)(B, S, D)),
            "shift_k": _w((lambda *s: torch.randn(*s, dtype=torch.bfloat16) * 0.2)(D, ks) * 0.5),
            "ssm_k": _w((lambda *s: torch.randn(*s, dtype=torch.bfloat16) * 0.8)(D, S) * (1.0 / S ** 0.5)),
            "ssm_D": _w((lambda *s: torch.randn(*s, dtype=torch.bfloat16) * 0.2)(D)),
        }
    _A_in = _A_setup()

    def _B_setup():
        B, S, D, F = 1, 2048, 512, 1376
        return {
            "x": _w((lambda *s: torch.randn(*s, dtype=torch.bfloat16) * 0.05)(B, S, D)),
            "w1": _w((lambda *s: torch.randn(*s, dtype=torch.bfloat16) * 3.2)(D, F) * (D ** -0.5)),
            "b1": _w((lambda *s: torch.randn(*s, dtype=torch.bfloat16) * 3.2)(F) * 0.01),
            "w2": _w((lambda *s: torch.randn(*s, dtype=torch.bfloat16) * 0.05)(F, D) * (F ** -0.5)),
            "b2": _w((lambda *s: torch.randn(*s, dtype=torch.bfloat16) * 0.1)(D) * 0.01),
        }
    _B_in = _B_setup()

    return {
        "A_q": _A_in["q"],
        "A_k": _A_in["k"],
        "A_v": _A_in["v"],
        "A_shift_k": _A_in["shift_k"],
        "A_ssm_k": _A_in["ssm_k"],
        "A_ssm_D": _A_in["ssm_D"],
        "B_x": _B_in["x"],
        "B_w1": _B_in["w1"],
        "B_b1": _B_in["b1"],
        "B_w2": _B_in["w2"],
        "B_b2": _B_in["b2"],
        "gate_w": _w((lambda *s: torch.randn(*s, dtype=torch.bfloat16) * 0.05)(512) * 16.0),
    }

def reference(A_q, A_k, A_v, A_shift_k, A_ssm_k, A_ssm_D, B_x, B_w1, B_b1, B_w2, B_b2, gate_w):
    def _A_ref(q, k, v, shift_k, ssm_k, ssm_D):
        B, S, D = q.shape
        ks = shift_k.shape[-1]
        k_pad = _pad(k, ((0, 0), (ks - 1, 0), (0, 0)))
        k_shift = torch.zeros_like(k)
        for t in range(ks):
            k_shift = k_shift + k_pad[:, t:t + S, :] * shift_k[:, ks - 1 - t][None, None, :]
        kv = k_shift * v
        L = 2 * S
        kv_pad = _pad(kv, ((0, 0), (0, S), (0, 0)))
        kf_pad = _pad(ssm_k, ((0, 0), (0, S)))
        kv_f = (lambda x, axes=(-2,-1): torch.fft.rfft2(x.float(), dim=axes))(kv_pad[:, None, :, :], axes=(1, 2))
        kf_f = (lambda x, axes=(-2,-1): torch.fft.rfft2(x.float(), dim=axes))(kf_pad[:, None, :], axes=(1, 2))
        kf_f = (lambda x, *d: x.permute(*d))(kf_f, 1, 2, 0)
        y_f = kv_f * kf_f[None, :, :, :]
        y = (lambda x, s, axes=(-2,-1): torch.fft.irfft2(x, s=s, dim=axes).to(torch.bfloat16))(y_f, (1, L), axes=(1, 2))[:, 0, :S, :]
        y = y + kv * ssm_D[None, None, :]
        return q * y

    def _B_ref(x, w1, b1, w2, b2):
        h = torch.einsum("bsd,df->bsf", x, w1) + b1
        h = _gelu(h)
        return torch.einsum("bsf,fd->bsd", h, w2) + b2

    x = A_q
    g = _sigmoid((x * gate_w).sum(-1, keepdims=True))
    a = _A_ref(x, A_k, A_v, A_shift_k, A_ssm_k, A_ssm_D)
    b = _B_ref(x, B_w1, B_b1, B_w2, B_b2)
    return g * a + (1 - g) * b
```

```cpp
#include <hip/hip_runtime.h>
#include <hip/hip_bf16.h>

typedef unsigned u32;
typedef unsigned long long u64;
typedef unsigned u4 __attribute__((ext_vector_type(4)));
typedef unsigned u2 __attribute__((ext_vector_type(2)));
typedef float f2 __attribute__((ext_vector_type(2)));
typedef float f4 __attribute__((ext_vector_type(4)));
typedef float f16v __attribute__((ext_vector_type(16)));
typedef __bf16 bf2 __attribute__((ext_vector_type(2)));
typedef __bf16 bf4 __attribute__((ext_vector_type(4)));
typedef __bf16 bf8 __attribute__((ext_vector_type(8)));
#define AS3 __attribute__((address_space(3)))
#define AS1 __attribute__((address_space(1)))
#define DEV __device__ __forceinline__

constexpr u32 WS_FKV = 4096, WS_FH = 8192, WS_FY = 12288;
constexpr u32 WS_KVT = 1u << 20, WS_YT = 4u << 20, WS_HF = 8u << 20, WS_TS = 16u << 20;
constexpr u32 SLOT = 28672, L_HC = 114688, L_U = L_HC + 33792, L_MISC = L_U + 10240;
constexpr u32 L_GW = L_MISC, L_GATE = L_MISC + 1024, L_EV = L_MISC + 1536;
constexpr u32 L_Z = 18432, L_P = 28672, LDS_BYTES = 163840;
enum { EV_KV = 0, EV_H, EV_W2A, EV_P1A, EV_P1B, EV_P2A, EV_P2B, EV_Y, EV_CD, EV_M, EV_W2B, EV_W2C, EV_Z };
constexpr int SPIN_MAX = 1 << 18;

DEV void dma16(u32 voff, const char* sb, u32 lds) {
  u32 keep;
  asm volatile("s_mov_b32 %0, m0\n\ts_mov_b32 m0, %3\n\ts_nop 0\n\tglobal_load_lds_dwordx4 %1, %2\n\ts_mov_b32 m0, %0"
               : "=&s"(keep) : "v"(voff), "s"(sb), "s"(lds) : "memory");
}
template <int N> DEV void vmwait() { asm volatile("s_waitcnt vmcnt(%0)" ::"n"(N) : "memory"); }
DEV void bar() { asm volatile("s_waitcnt lgkmcnt(0)\n\ts_barrier" ::: "memory"); }
DEV u4 lds128(u32 a) { return *(AS3 u4*)(size_t)a; }
DEV void sts128(u32 a, u4 v) { *(AS3 u4*)(size_t)a = v; }
DEV bf4 ldtr(u32 a) { return __builtin_amdgcn_ds_read_tr16_b64_v4bf16((AS3 bf4*)(size_t)a); }
DEV bf8 cat(bf4 a, bf4 b) { return __builtin_shufflevector(a, b, 0, 1, 2, 3, 4, 5, 6, 7); }
DEV float blo(u32 v) { return __builtin_bit_cast(float, v << 16); }
DEV float bhi(u32 v) { return __builtin_bit_cast(float, v & 0xffff0000u); }
DEV u32 pk(float a, float b) { f2 t = {a, b}; return __builtin_bit_cast(u32, __builtin_convertvector(t, bf2)); }
DEV u32 ev_add(int e) {
  u32 old = 0;
  asm volatile("s_waitcnt lgkmcnt(0)" ::: "memory");
  if ((threadIdx.x & 63) == 0)
    old = __hip_atomic_fetch_add((AS3 u32*)(size_t)(L_EV + 4 * e), 1u, __ATOMIC_RELAXED, __HIP_MEMORY_SCOPE_WORKGROUP);
  old = __builtin_amdgcn_readfirstlane(old);
  asm volatile("" ::: "memory");
  return old;
}
DEV void ev_wait(int e, u32 n) {
  asm volatile("" ::: "memory");
#pragma unroll 1
  for (int it = 0; it < SPIN_MAX; ++it) {
    u32 v = *(volatile AS3 u32*)(size_t)(L_EV + 4 * e);
    if (__builtin_amdgcn_readfirstlane(v) >= n) break;
    __builtin_amdgcn_s_sleep(1);
  }
  asm volatile("" ::: "memory");
}
DEV u64 ld_flag(const char* p) { return __hip_atomic_load((AS1 u64*)p, __ATOMIC_RELAXED, __HIP_MEMORY_SCOPE_AGENT); }
DEV void st_flag(char* p, u64 v) { __hip_atomic_store((AS1 u64*)p, v, __ATOMIC_RELAXED, __HIP_MEMORY_SCOPE_AGENT); }
DEV void poll_from(u64 v, const char* p, u64 tag) {
#pragma unroll 1
  for (int it = 0; it < SPIN_MAX; ++it) {
    if (__builtin_amdgcn_ballot_w64(v != tag) == 0) break;
    __builtin_amdgcn_s_sleep(2);
    v = ld_flag(p);
  }
  asm volatile("" ::: "memory");
}
#define STAMP(i)

__global__ __launch_bounds__(512, 2) void fused_kernel(
    const char* __restrict__ q, const char* __restrict__ kk, const char* __restrict__ vv, const char* __restrict__ shk,
    const char* __restrict__ ssk, const char* __restrict__ ssd, const char* __restrict__ w1, const char* __restrict__ b1,
    const char* __restrict__ w2, const char* __restrict__ b2, const char* __restrict__ gw, char* __restrict__ out,
    char* __restrict__ ws) {
  const int b = blockIdx.x, l = threadIdx.x & 63;
  const int w = __builtin_amdgcn_readfirstlane(threadIdx.x >> 6);
  const int cg = b >> 5, rb = (b & 7) * 4 + ((b >> 3) & 3);
  const int R = rb >> 1, g = (rb & 1) * 8 + cg;
  const int tau0 = g < 11 ? 3 * g : 33 + 2 * (g - 11);
  const bool three = g < 11;
  const int w4 = w & 3;
  const __amdgpu_buffer_rsrc_t rws = __builtin_amdgcn_make_buffer_rsrc(ws, 0, 32u << 20, 0x00020000);
  u32 xo[2], wo[3];
#pragma unroll
  for (int p = 0; p < 2; ++p) xo[p] = (l >> 3) * 1024 + (((l & 7) ^ (4 * p + (l >> 4))) << 4);
#pragma unroll
  for (int j = 0; j < 3; ++j) {
    int n = (3 * w4 + j) * 64 + l, row = n / 12, c = n % 12;
    if (!three && c >= 8) c -= 8;
    wo[j] = row * 2752 + c * 16;
  }
  const char* xb = q + (size_t)(R * 128 + w4 * 32) * 1024;
  const char* w1b = w1 + tau0 * 64;
  auto dma_chunk = [&](int c) {
    const u32 s = (c & 3) * SLOT;
#pragma unroll
    for (int j = 0; j < 4; ++j) dma16(xo[j & 1], xb + j * 8192 + c * 128, s + (4 * w4 + j) * 1024);
#pragma unroll
    for (int j = 0; j < 3; ++j) dma16(wo[j], w1b + c * (64 * 2752), s + 16384 + (3 * w4 + j) * 1024);
  };
  STAMP(0);
  u32 zoff = 0;
  asm volatile("" : "+v"(zoff));
  const u64 e0 = ld_flag(ws + zoff);

  if (w < 4) {
    const int n = l & 31, hh = l >> 5, G = l >> 4, tq = (l >> 2) & 3, tp = l & 3;
    __builtin_amdgcn_s_setprio(1);
    dma_chunk(0); dma_chunk(1); dma_chunk(2); dma_chunk(3);
    u2 bb[3][4];
    f16v acc[3];
#pragma unroll
    for (int t = 0; t < 3; ++t)
#pragma unroll
      for (int i = 0; i < 16; ++i) acc[t][i] = 0.f;
    u64 tag = 0;
    STAMP(1);
    const u32 xrd = (32 * w + n) * 128, xsw = (n >> 1) & 7;
    const u32 wrd = 16384 + (8 * hh + tq) * 192 + (G & 1) * 32 + tp * 8;
    u64 fv = 0; u4 uu[2];
    const char* fkv = ws + WS_FKV + ((b >> 5) * 32 + n) * 8;
    u32 xa[4];
#pragma unroll
    for (int k = 0; k < 4; ++k) xa[k] = xrd + (((2 * k + hh) ^ xsw) << 4);
    vmwait<21>();
    bar();
    STAMP(2);
    u4 X = lds128(xa[0]);
    bf8 A[3];
#pragma unroll
    for (int t = 0; t < 3; ++t) A[t] = cat(ldtr(wrd + t * 64), ldtr(wrd + 768 + t * 64));
#pragma unroll
    for (int ks = 0; ks < 32; ++ks) {
      const int c = ks >> 2, k = ks & 3;
      if (k == 3) {
        if (c == 0) vmwait<14>();
        if (c == 1) vmwait<7>();
        if (c == 2) vmwait<0>();
        if (c == 7) {
#pragma unroll
          for (int e = 0; e < 2; ++e) {
            int p = (w & 1) * 128 + e * 64 + l;
            sts128(L_U + (w >> 1) * 5120 + (p & 3) * 1280 + ((p >> 2) + 16) * 16, uu[e]);
          }
          if (w < 2) sts128(L_U + (w * 4 + (l >> 4)) * 1280 + (l & 15) * 16, u4{0, 0, 0, 0});
        }
        bar();
        if (c == 2) {
          tag = e0 * 6364136223846793005ull + 1442695040888963407ull;
#pragma unroll
          for (int t = 0; t < 3; ++t)
#pragma unroll
            for (int i = 0; i < 4; ++i)
              bb[t][i] = *(const u2*)(b1 + ((tau0 + ((t == 2 && !three) ? 0 : t)) * 32 + 8 * i + 4 * hh) * 2);
        }
        if (c == 3) STAMP(3);
        if (c == 4) fv = ld_flag(fkv);
        if (c == 5) {
          poll_from(fv, fkv, tag);
          STAMP(4);
#pragma unroll
          for (int e = 0; e < 2; ++e)
            uu[e] = __builtin_amdgcn_raw_buffer_load_b128(rws, (2 * b + (w >> 1)) * 4096 + ((w & 1) * 128 + e * 64 + l) * 16, WS_KVT, 16);
        }
      }
      const int kn = (ks + 1) & 3;
      const u32 sn = (((ks + 1) >> 2) & 3) * SLOT;
      const u4 Xc = X;
      if (ks < 31) X = lds128(sn + xa[kn]);
#pragma unroll
      for (int t = 0; t < 3; ++t) {
        const bf8 Ac = A[t];
        if (ks < 31) A[t] = cat(ldtr(sn + wrd + kn * 3072 + t * 64), ldtr(sn + wrd + kn * 3072 + 768 + t * 64));
        __builtin_amdgcn_sched_barrier(0);
        acc[t] = __builtin_amdgcn_mfma_f32_32x32x16_bf16(Ac, __builtin_bit_cast(bf8, Xc), acc[t], 0, 0, 0);
        __builtin_amdgcn_sched_barrier(0);
      }
    }
    STAMP(5);
    {
      const float c0 = -2.f * 0.7978845608028654f * 1.4426950408889634f, c1 = c0 * 0.044715f;
      const u32 hb = ((4 * R + w) * 86 + 2 * tau0) * 1024 + l * 16;
#pragma unroll
      for (int t = 0; t < 3; ++t) {
        if (t < 2 || three) {
#pragma unroll
          for (int s = 0; s < 2; ++s) {
            u4 o;
#pragma unroll
            for (int i = 0; i < 4; ++i) {
              const u32 bw = bb[t][2 * s + (i >> 1)][i & 1];
              f2 x = {acc[t][8 * s + 2 * i] + blo(bw), acc[t][8 * s + 2 * i + 1] + bhi(bw)};
              f2 u = x * (x * x * c1 + c0);
              f2 d = {__builtin_amdgcn_exp2f(u[0]), __builtin_amdgcn_exp2f(u[1])};
              d += 1.f;
              f2 r = {__builtin_amdgcn_rcpf(d[0]), __builtin_amdgcn_rcpf(d[1])};
              r *= x;
              o[i] = pk(r[0], r[1]);
            }
            __builtin_amdgcn_raw_buffer_store_b128(o, rws, WS_HF + hb + (2 * t + s) * 1024, 0, 16);
          }
        }
      }
    }
    STAMP(6);
    vmwait<0>();
    if (ev_add(EV_H) == 3 && l == 0) st_flag(ws + WS_FH + (R * 16 + g) * 8, tag);
    STAMP(7);
    const char* fh = ws + WS_FH + (R * 16 + (l & 15)) * 8;
    poll_from(ld_flag(fh), fh, tag);
    STAMP(8);
    const int tt = w & 1, kh = w >> 1;
    const u32 hsrc = ((2 * rb + tt) * 86 + kh) * 1024 + l * 16;
    constexpr int PF = 10;
    u4 hf[43];
#pragma unroll
    for (int i = 0; i < PF; ++i) hf[i] = __builtin_amdgcn_raw_buffer_load_b128(rws, hsrc, WS_HF + i * 2048, 16);
    const u32 c16 = (G & 1) * 2 + (tp >> 1);
    const u32 ard = kh * 2048 + (4 * hh + tq) * 128 + (tp & 1) * 8;
    const u32 a0 = ard + ((c16 ^ ((tq >> 1) << 2)) << 4), a1 = ard + (((c16 + 4) ^ ((tq >> 1) << 2)) << 4);
    f16v d0, d1;
#pragma unroll
    for (int i = 0; i < 16; ++i) { d0[i] = 0.f; d1[i] = 0.f; }
    ev_wait(EV_W2A, 4);
    STAMP(9);
    bf8 A0 = cat(ldtr(a0), ldtr(a0 + 1024)), A1 = cat(ldtr(a1), ldtr(a1 + 1024));
#pragma unroll
    for (int i = 0; i < 43; ++i) {
      if (i == 27) { STAMP(10); ev_wait(EV_W2B, 4); }
      if (i == 37) { STAMP(11); ev_wait(EV_W2C, 4); }
      if (i + PF < 43) hf[i + PF] = __builtin_amdgcn_raw_buffer_load_b128(rws, hsrc, WS_HF + (i + PF) * 2048, 16);
      bf8 N0 = A0, N1 = A1;
      if (i < 42) {
        const u32 ro = (i + 1 < 38 ? i + 1 : i + 1 - 38) * 4096;
        N0 = cat(ldtr(a0 + ro), ldtr(a0 + ro + 1024));
        N1 = cat(ldtr(a1 + ro), ldtr(a1 + ro + 1024));
      }
      __builtin_amdgcn_sched_barrier(0);
      d0 = __builtin_amdgcn_mfma_f32_32x32x16_bf16(A0, __builtin_bit_cast(bf8, hf[i]), d0, 0, 0, 0);
      d1 = __builtin_amdgcn_mfma_f32_32x32x16_bf16(A1, __builtin_bit_cast(bf8, hf[i]), d1, 0, 0, 0);
      if (i == 16) ev_add(EV_M);
      __builtin_amdgcn_sched_barrier(0);
      A0 = N0; A1 = N1;
    }
    STAMP(12);
    const u32 pw = L_P + 2048 + kh * 17408 + (32 * tt + n) * 272 + hh * 16;
#pragma unroll
    for (int i = 0; i < 4; ++i) {
      sts128(pw + i * 32, __builtin_bit_cast(u4, f4{d0[4 * i], d0[4 * i + 1], d0[4 * i + 2], d0[4 * i + 3]}));
      sts128(pw + 128 + i * 32, __builtin_bit_cast(u4, f4{d1[4 * i], d1[4 * i + 1], d1[4 * i + 2], d1[4 * i + 3]}));
    }
    bar();
  } else {
    const int hw = w4, ht = hw * 64 + l;
    {
      const int rg = ht >> 3, c8 = ht & 7;
      const __amdgpu_buffer_rsrc_t rk = __builtin_amdgcn_make_buffer_rsrc((void*)kk, 0, 2048 * 1024, 0x00020000);
      const int off = (64 * rb + 2 * rg) * 1024 + cg * 128 + c8 * 16;
      u4 kr[5], vr[2], sk[4];
#pragma unroll
      for (int j = 0; j < 5; ++j) {
        int o = off + (j - 3) * 1024;
        kr[j] = __builtin_amdgcn_raw_buffer_load_b128(rk, o < 0 ? 0x40000000 : o, 0, 0);
      }
#pragma unroll
      for (int j = 0; j < 2; ++j) vr[j] = *(const u4*)(vv + off + j * 1024);
#pragma unroll
      for (int j = 0; j < 4; ++j) sk[j] = *(const u4*)(shk + (cg * 64 + c8 * 8) * 8 + j * 16);
      if (ht < 16) *(AS3 u32*)(size_t)(L_EV + ht * 4) = 0;
#pragma unroll
      for (int e = 0; e < 8; ++e) {
        const u32 sa = sk[e >> 1][(e & 1) * 2], sb = sk[e >> 1][(e & 1) * 2 + 1];
        const float s0 = blo(sa), s1 = bhi(sa), s2 = blo(sb), s3 = bhi(sb);
        float kf[5], vf[2];
#pragma unroll
        for (int j = 0; j < 5; ++j) kf[j] = (e & 1) ? bhi(kr[j][e >> 1]) : blo(kr[j][e >> 1]);
#pragma unroll
        for (int j = 0; j < 2; ++j) vf[j] = (e & 1) ? bhi(vr[j][e >> 1]) : blo(vr[j][e >> 1]);
        const float r0 = (kf[0] * s3 + kf[1] * s2 + kf[2] * s1 + kf[3] * s0) * vf[0];
        const float r1 = (kf[1] * s3 + kf[2] * s2 + kf[3] * s1 + kf[4] * s0) * vf[1];
        *(AS3 u32*)(size_t)(L_U + c8 * 1056 + e * 128 + rg * 4) = pk(r0, r1);
      }
    }
    u64 tag = e0 * 6364136223846793005ull + 1442695040888963407ull;
    asm volatile("" : "+v"(tag));
    const int ci = hw >> 1, ii = ht & 127;
    u4 hk[3], qr[2], br[2];
    {
      const __amdgpu_buffer_rsrc_t rs = __builtin_amdgcn_make_buffer_rsrc((void*)ssk, 0, 512 * 4096, 0x00020000);
      const int o = (2 * b + ci) * 4096 + ii * 32;
      hk[0] = __builtin_amdgcn_raw_buffer_load_b128(rs, ii == 0 ? 0x40000000 : o - 16, 0, 0);
      hk[1] = __builtin_amdgcn_raw_buffer_load_b128(rs, o, 0, 0);
      hk[2] = __builtin_amdgcn_raw_buffer_load_b128(rs, o + 16, 0, 0);
    }
    const int orow = 64 * rb + 16 * hw + (l & 15), grp = l >> 4;
#pragma unroll
    for (int e = 0; e < 2; ++e) {
      qr[e] = *(const u4*)(q + orow * 1024 + cg * 128 + (grp + 4 * e) * 16);
      br[e] = *(const u4*)(b2 + cg * 128 + (grp + 4 * e) * 16);
    }
    u32 dd = *(const u32*)(ssd + 4 * b);
    float gacc = 0.f;
    const u32 gx = ((rb & 1) * 64 + l) * 128, gs = (l >> 1) & 7;
    auto gate = [&](int c) {
#pragma unroll
      for (int e = 0; e < 2; ++e) {
        const u4 xv = lds128((c & 3) * SLOT + gx + (((2 * hw + e) ^ gs) << 4));
        const u4 gv = *(const u4*)(gw + c * 128 + hw * 32 + e * 16);
#pragma unroll
        for (int i = 0; i < 4; ++i) {
          const u32 xx = xv[i], gg = gv[i];
          gacc = __builtin_amdgcn_fdot2_f32_bf16(__builtin_bit_cast(bf2, gg), __builtin_bit_cast(bf2, xx), gacc, false);
        }
      }
      asm volatile("" : "+v"(gacc));
    };
    STAMP(1);
    bar();
    STAMP(2);
#pragma unroll
    for (int e = 0; e < 2; ++e) {
      const int ch = 16 * hw + 8 * e + (l >> 3);
      u4 v = lds128(L_U + (ch >> 3) * 1056 + (ch & 7) * 128 + (l & 7) * 16);
      __builtin_amdgcn_raw_buffer_store_b128(v, rws, WS_KVT + (64 * cg + ch) * 4096 + rb * 128 + (l & 7) * 16, 0, 16);
    }
    {
      u32 W[13];
#pragma unroll
      for (int j = 0; j < 12; ++j) W[j] = hk[j >> 2][j & 3];
      W[12] = 0;
      u32 A[12];
#pragma unroll
      for (int j = 0; j < 12; ++j) A[j] = __builtin_amdgcn_alignbit(W[j + 1], W[j], 16);
      const u32 p = L_HC + ci * 16896 + 64 + ii * 32;
      sts128(p, u4{W[4], W[5], W[6], W[7]});                 sts128(p + 16, u4{W[8], W[9], W[10], W[11]});
      sts128(p + 4224, u4{W[3], W[4], W[5], W[6]});          sts128(p + 4240, u4{W[7], W[8], W[9], W[10]});
      sts128(p + 8448, u4{A[1], A[2], A[3], A[4]});          sts128(p + 8464, u4{A[5], A[6], A[7], A[8]});
      sts128(p + 12672, u4{A[0], A[1], A[2], A[3]});         sts128(p + 12688, u4{A[4], A[5], A[6], A[7]});
      if (ii == 127) {
        sts128(p + 32, u4{0, 0, 0, 0});                      sts128(p + 4224 + 32, u4{W[11], 0, 0, 0});
        sts128(p + 8448 + 32, u4{A[9], A[10], A[11], 0});    sts128(p + 12672 + 32, u4{A[8], A[9], A[10], A[11]});
      }
      if (ii < 16) sts128(L_HC + ci * 16896 + (ii >> 2) * 4224 + (ii & 3) * 16, u4{0, 0, 0, 0});
    }
    asm volatile("" : "+v"(qr[0]), "+v"(qr[1]), "+v"(br[0]), "+v"(br[1]), "+v"(dd));
    const u32 w2o = (l >> 3) * 1024 + (((l & 7) ^ (((l >> 4) & 1) << 2)) << 4);
    const char* w2b = w2 + cg * 128;
    auto dma_w2 = [&](int j) { dma16(w2o, w2b + j * 8192, (j < 152 ? j : j - 152) * 1024); };
    gate(0);
    bar();
    dma_chunk(4);
    gate(1);
    vmwait<7>();
    if (ev_add(EV_KV) == 3 && l == 0) st_flag(ws + WS_FKV + (cg * 32 + rb) * 8, tag);
    STAMP(3);
    bar();
    dma_chunk(5);
    gate(2);
    bar();
    dma_chunk(6);
    gate(3);
    vmwait<14>();
    bar();
    dma_chunk(7);
    gate(4);
    vmwait<14>();
#pragma unroll
    for (int s = 0; s < 4; ++s) {
      bar();
      if (s == 3) STAMP(4);
#pragma unroll
      for (int j = 0; j < 7; ++j) dma_w2(28 * s + 7 * hw + j);
      if (s < 3) gate(5 + s);
      if (s == 2) *(AS3 float*)(size_t)(L_GW + ht * 4) = gacc;
      if (s < 2) vmwait<14>();
    }
    STAMP(5);
    {
      const int hf = hw & 1, G = l >> 4, tq = (l >> 2) & 3, tp = l & 3;
      const u32 ab = L_HC + ci * 16896 + (tq & 1) * 8448 + (tq >> 1) * 4224 + (8 * tp - 8 * G + 28 + (tq & 1) * 4 + 256 * hf) * 2;
      const u32 ub = L_U + ci * 5120 + G * 1280 + ((l & 15) + 9 - 8 * hf) * 16;
      f4 y[4][2];
#pragma unroll
      for (int j = 0; j < 4; ++j) { y[j][0] = f4{0, 0, 0, 0}; y[j][1] = f4{0, 0, 0, 0}; }
      u4 Bn[4]; bf4 An[12];
      auto rd = [&](int st, int m) {
        if (m < 3) An[m] = ldtr(ab + 64 * st + 8 * m);
        else if (m < 7) Bn[m - 3] = lds128(ub + (16 * (m - 3) + 7 - st) * 16);
        else An[m - 4] = ldtr(ab + 1024 * ((m - 4) / 3) + 64 * st + 8 * ((m - 4) % 3));
      };
#pragma unroll
      for (int m = 0; m < 16; ++m) rd(0, m);
#pragma unroll
      for (int st = 0; st < 8; ++st) {
        u4 B[4]; bf4 A[12];
#pragma unroll
        for (int j = 0; j < 4; ++j) B[j] = Bn[j];
#pragma unroll
        for (int j = 0; j < 12; ++j) A[j] = An[j];
        int m = 0;
#pragma unroll
        for (int i = 0; i < 4; ++i) {
#pragma unroll
          for (int j = 0; j + i < 4; ++j)
#pragma unroll
            for (int r = 0; r < 2; ++r) {
              __builtin_amdgcn_sched_barrier(0);
              y[j + i][r] = __builtin_amdgcn_mfma_f32_16x16x32_bf16(cat(A[3 * i + 1 + r], A[3 * i + r]),
                                                                   __builtin_bit_cast(bf8, B[j]), y[j + i][r], 0, 0, 0);
              __builtin_amdgcn_sched_barrier(0);
              if (st < 7 && m < 16) rd(st + 1, m);
              ++m;
            }
        }
        if (st == 3) { vmwait<0>(); ev_add(EV_W2A); }
      }
      STAMP(6);
      ev_add(EV_P1A + ci); ev_wait(EV_P1A + ci, 2);
      const u32 xa = L_HC + ci * 16896;
#pragma unroll
      for (int j = 0; j < 4; ++j)
#pragma unroll
        for (int r = 0; r < 2; ++r)
          sts128(xa + hf * 8192 + (16 * j + (l & 15)) * 128 + (((2 * G + r) ^ (l & 7)) << 4), __builtin_bit_cast(u4, y[j][r]));
      ev_add(EV_P2A + ci); ev_wait(EV_P2A + ci, 2);
      const float D = ci ? bhi(dd) : blo(dd);
      u4 zo[2];
#pragma unroll
      for (int e = 0; e < 2; ++e) {
        const int j = 2 * hf + e;
        u4 u = lds128(L_U + ci * 5120 + (l & 3) * 1280 + (16 * j + (l >> 2) + 16) * 16);
#pragma unroll
        for (int r = 0; r < 2; ++r) {
          const u32 a = xa + (16 * j + (l >> 2)) * 128 + (((2 * (l & 3) + r) ^ ((l >> 2) & 7)) << 4);
          f4 s = __builtin_bit_cast(f4, lds128(a)) + __builtin_bit_cast(f4, lds128(a + 8192));
          zo[e][2 * r] = pk(s[0] + D * blo(u[2 * r]), s[1] + D * bhi(u[2 * r]));
          zo[e][2 * r + 1] = pk(s[2] + D * blo(u[2 * r + 1]), s[3] + D * bhi(u[2 * r + 1]));
        }
        __builtin_amdgcn_raw_buffer_store_b128(zo[e], rws, WS_YT + (2 * b + ci) * 4096 + j * 1024 + l * 16, 0, 16);
      }
    }
    ev_add(EV_CD); ev_wait(EV_CD, 4);
    STAMP(7);
#pragma unroll
    for (int j = 0; j < 10; ++j) dma_w2(112 + 10 * hw + j);
    vmwait<10>();
    if (ev_add(EV_Y) == 3 && l == 0) st_flag(ws + WS_FY + b * 8, tag);
    vmwait<0>();
    ev_add(EV_W2B);
    STAMP(8);
    ev_wait(EV_M, 4);
#pragma unroll
    for (int j = 0; j < 5; ++j) dma_w2(152 + 5 * hw + j);
    const char* fy = ws + WS_FY + (32 * cg + 8 * hw + (l & 7)) * 8;
    u64 fv = ld_flag(fy);
    vmwait<0>();
    ev_add(EV_W2C);
    poll_from(fv, fy, tag);
    STAMP(9);
#pragma unroll
    for (int e = 0; e < 2; ++e) {
      const int ch = 16 * hw + 8 * e + (l >> 3);
      u4 v = __builtin_amdgcn_raw_buffer_load_b128(rws, (64 * cg + ch) * 4096 + rb * 128 + (l & 7) * 16, WS_YT, 16);
      sts128(L_Z + 2048 + ch * 144 + (l & 7) * 16, v);
    }
    if (b == 0 && hw == 0) {
#pragma unroll
      for (int j = 0; j < 4; ++j) {
        const char* p = ws + WS_FKV + (j * 64 + l) * 8;
        poll_from(ld_flag(p), p, tag);
      }
      if (l == 0) st_flag(ws, tag);
    }
    ev_add(EV_Z); ev_wait(EV_Z, 4);
    float gl = 0.f;
#pragma unroll
    for (int j = 0; j < 4; ++j) gl += *(AS3 float*)(size_t)(L_GW + (64 * j + 16 * hw + (l & 15)) * 4);
    const float gt = __builtin_amdgcn_rcpf(1.f + __builtin_amdgcn_exp2f(-1.4426950408889634f * gl));
    float ap[2][8];
#pragma unroll
    for (int e = 0; e < 2; ++e) {
      const u32 a = L_Z + 2048 + (8 * (grp + 4 * e) + ((l >> 2) & 3)) * 144 + (16 * hw + 4 * (l & 3)) * 2;
      u4 z = __builtin_bit_cast(u4, cat(ldtr(a), ldtr(a + 4 * 144)));
#pragma unroll
      for (int i = 0; i < 4; ++i) {
        ap[e][2 * i] = gt * blo(qr[e][i]) * blo(z[i]) + (1.f - gt) * blo(br[e][i]);
        ap[e][2 * i + 1] = gt * bhi(qr[e][i]) * bhi(z[i]) + (1.f - gt) * bhi(br[e][i]);
      }
    }
    STAMP(10);
    bar();
    STAMP(11);
#pragma unroll
    for (int e = 0; e < 2; ++e) {
      const u32 a = L_P + 2048 + (16 * hw + (l & 15)) * 272 + (grp + 4 * e) * 32;
      u4 o;
#pragma unroll
      for (int r = 0; r < 2; ++r) {
        f4 s = __builtin_bit_cast(f4, lds128(a + r * 16)) + __builtin_bit_cast(f4, lds128(a + 17408 + r * 16));
        o[2 * r] = pk(ap[e][4 * r] + (1.f - gt) * s[0], ap[e][4 * r + 1] + (1.f - gt) * s[1]);
        o[2 * r + 1] = pk(ap[e][4 * r + 2] + (1.f - gt) * s[2], ap[e][4 * r + 3] + (1.f - gt) * s[3]);
      }
      *(u4*)(out + orow * 1024 + cg * 128 + (grp + 4 * e) * 16) = o;
    }
    STAMP(12);
  }
}

extern "C" void kernel_launch(void* const* d_in, const int* in_sizes, int n_in, void* d_out, int out_size, void* d_ws,
                              size_t ws_size, hipStream_t stream) {
  (void)in_sizes; (void)n_in; (void)out_size; (void)ws_size;
  (void)hipFuncSetAttribute((const void*)fused_kernel, hipFuncAttributeMaxDynamicSharedMemorySize, LDS_BYTES);
  fused_kernel<<<256, 512, LDS_BYTES, stream>>>(
      (const char*)d_in[0], (const char*)d_in[1], (const char*)d_in[2], (const char*)d_in[3], (const char*)d_in[4],
      (const char*)d_in[5], (const char*)d_in[7], (const char*)d_in[8], (const char*)d_in[9], (const char*)d_in[10],
      (const char*)d_in[11], (char*)d_out, (char*)d_ws);
}
```

```cpp
#include <hip/hip_runtime.h>
#include <hip/hip_bf16.h>

typedef unsigned u32;
typedef unsigned long long u64;
typedef unsigned u4 __attribute__((ext_vector_type(4)));
typedef unsigned u2 __attribute__((ext_vector_type(2)));
typedef float f2 __attribute__((ext_vector_type(2)));
typedef float f4 __attribute__((ext_vector_type(4)));
typedef float f16v __attribute__((ext_vector_type(16)));
typedef __bf16 bf2 __attribute__((ext_vector_type(2)));
typedef __bf16 bf4 __attribute__((ext_vector_type(4)));
typedef __bf16 bf8 __attribute__((ext_vector_type(8)));
#define AS3 __attribute__((address_space(3)))
#define AS1 __attribute__((address_space(1)))
#define AS4 __attribute__((address_space(4)))
#define DEV __device__ __forceinline__

constexpr u32 WS_FKV = 4096, WS_FH = 8192, WS_FY = 12288;
constexpr u32 WS_KVT = 1u << 20, WS_YT = 4u << 20, WS_HF = 8u << 20, WS_TS = 16u << 20;
constexpr u32 SLOT = 28672, L_HC = 114688, L_U = L_HC + 33792, L_MISC = L_U + 10240;
constexpr u32 L_GW = L_MISC, L_B = L_MISC + 1024, L_EV = L_MISC + 1536;
constexpr u32 L_Z = 18432, L_P = 28672, LDS_BYTES = 163840;
enum { EV_KV = 0, EV_H, EV_W2A, EV_PA, EV_PB, EV_U, EV_Y, EV_CD, EV_M, EV_W2B, EV_W2C, EV_Z, EV_G };
#define PSLEEP 8
constexpr int SPIN_MAX = 1 << 18;

DEV void dma16(u32 voff, const char* sb, u32 lds) {
  u32 keep;
  asm volatile("s_mov_b32 %0, m0\n\ts_mov_b32 m0, %3\n\ts_nop 0\n\tglobal_load_lds_dwordx4 %1, %2\n\ts_mov_b32 m0, %0"
               : "=&s"(keep) : "v"(voff), "s"(sb), "s"(lds) : "memory");
}
DEV u4 gld(u32 voff, const char* sb) {
  u4 r; asm volatile("global_load_dwordx4 %0, %1, %2" : "=v"(r) : "v"(voff), "s"(sb) : "memory"); return r;
}
DEV u4 gld_sc1(u32 voff, const char* sb) {
  u4 r; asm volatile("global_load_dwordx4 %0, %1, %2 sc1" : "=v"(r) : "v"(voff), "s"(sb) : "memory"); return r;
}
DEV u64 gld8_sc1(u32 voff, const char* sb) {
  u64 r; asm volatile("global_load_dwordx2 %0, %1, %2 sc1" : "=v"(r) : "v"(voff), "s"(sb) : "memory"); return r;
}
#define TIE(...) asm volatile("" : __VA_ARGS__)
DEV float dot2(u32 a, u32 b, float c) {
  return __builtin_amdgcn_fdot2_f32_bf16(__builtin_bit_cast(bf2, a), __builtin_bit_cast(bf2, b), c, false);
}
template <int N> DEV void vmwait() { asm volatile("s_waitcnt vmcnt(%0)" ::"n"(N) : "memory"); }
DEV void bar() { asm volatile("s_waitcnt lgkmcnt(0)\n\ts_barrier" ::: "memory"); }
DEV u4 lds128(u32 a) { return *(AS3 u4*)(size_t)a; }
DEV void sts128(u32 a, u4 v) { *(AS3 u4*)(size_t)a = v; }
DEV bf4 ldtr(u32 a) { return __builtin_amdgcn_ds_read_tr16_b64_v4bf16((AS3 bf4*)(size_t)a); }
DEV bf8 cat(bf4 a, bf4 b) { return __builtin_shufflevector(a, b, 0, 1, 2, 3, 4, 5, 6, 7); }
DEV float blo(u32 v) { return __builtin_bit_cast(float, v << 16); }
DEV float bhi(u32 v) { return __builtin_bit_cast(float, v & 0xffff0000u); }
DEV u32 pk(float a, float b) { f2 t = {a, b}; return __builtin_bit_cast(u32, __builtin_convertvector(t, bf2)); }
#define NOPK(x)
DEV u32 ev_add(int e) {
  u32 old = 0;
  asm volatile("s_waitcnt lgkmcnt(0)" ::: "memory");
  if ((threadIdx.x & 63) == 0)
    old = __hip_atomic_fetch_add((AS3 u32*)(size_t)(L_EV + 4 * e), 1u, __ATOMIC_RELAXED, __HIP_MEMORY_SCOPE_WORKGROUP);
  old = __builtin_amdgcn_readfirstlane(old);
  asm volatile("" ::: "memory");
  return old;
}
DEV void ev_wait(int e, u32 n) {
  asm volatile("" ::: "memory");
#pragma unroll 1
  for (int it = 0; it < SPIN_MAX; ++it) {
    u32 v = *(volatile AS3 u32*)(size_t)(L_EV + 4 * e);
    if (__builtin_amdgcn_readfirstlane(v) >= n) break;
    __builtin_amdgcn_s_sleep(1);
  }
  asm volatile("" ::: "memory");
}
DEV u64 ld_flag(const char* p) { return __hip_atomic_load((AS1 u64*)p, __ATOMIC_RELAXED, __HIP_MEMORY_SCOPE_AGENT); }
DEV void st_flag(char* p, u64 v) { __hip_atomic_store((AS1 u64*)p, v, __ATOMIC_RELAXED, __HIP_MEMORY_SCOPE_AGENT); }
DEV void poll_from(u64 v, const char* p, u64 tag) {
#pragma unroll 1
  for (int it = 0; it < SPIN_MAX; ++it) {
    if (__builtin_amdgcn_ballot_w64(v != tag) == 0) break;
    __builtin_amdgcn_s_sleep(PSLEEP);
    v = ld_flag(p);
  }
  asm volatile("" ::: "memory");
}
#define STAMP(i)
#define DUMP()

__global__ __launch_bounds__(512, 2) void fused_kernel(
    const char* __restrict__ q, const char* __restrict__ kk, const char* __restrict__ vv, const char* __restrict__ shk,
    const char* __restrict__ ssk, const char* __restrict__ ssd, const char* __restrict__ w1, const char* __restrict__ b1,
    const char* __restrict__ w2, const char* __restrict__ b2, const char* __restrict__ gw, char* __restrict__ out,
    char* __restrict__ ws) {
  STAMP(0);
  asm volatile("" ::"s"(q), "s"(kk), "s"(vv), "s"(shk), "s"(ssk), "s"(ssd), "s"(w1), "s"(b1), "s"(w2), "s"(b2), "s"(gw), "s"(out), "s"(ws));
  const int b = blockIdx.x, l = threadIdx.x & 63;
  const int w = __builtin_amdgcn_readfirstlane(threadIdx.x >> 6);
  const int cg = b >> 5, rb = (b & 7) * 4 + ((b >> 3) & 3);
  const int R = rb >> 1, g = (rb & 1) * 8 + cg;
  const int tau0 = g < 11 ? 3 * g : 33 + 2 * (g - 11);
  const bool three = g < 11;
  const int w4 = w & 3;
  const __amdgpu_buffer_rsrc_t rws = __builtin_amdgcn_make_buffer_rsrc(ws, 0, 32u << 20, 0x00020000);
  u32 xo[2], wo[3];
#pragma unroll
  for (int p = 0; p < 2; ++p) xo[p] = (l >> 3) * 1024 + (((l & 7) ^ (4 * p + (l >> 4))) << 4);
#pragma unroll
  for (int j = 0; j < 3; ++j) {
    int n = (3 * w4 + j) * 64 + l, row = n / 12, c = n % 12;
    if (!three && c >= 8) c -= 8;
    wo[j] = row * 2752 + c * 16;
  }
  const char* xb = q + (size_t)(R * 128 + w4 * 32) * 1024;
  const char* w1b = w1 + tau0 * 64;
  const int rot1 = (cg + 4 * (R & 1)) & 7;
  const int rot2 = (5 * cg + 11 * ((b >> 3) & 3)) % 43;
  auto dma_piece = [&](int c, int j) {
    const u32 s = (c & 3) * SLOT;
    const int pc = (c + rot1) & 7;
    if (j < 4) dma16(xo[j & 1], xb + j * 8192 + pc * 128, s + (4 * w4 + j) * 1024);
    else dma16(wo[j - 4], w1b + pc * (64 * 2752), s + 16384 + (3 * w4 + j - 4) * 1024);
  };
  auto dma_chunk = [&](int c) {
#pragma unroll
    for (int j = 0; j < 7; ++j) dma_piece(c, j);
  };
  u64 e0 = gld8_sc1(0, ws);

  if (w < 4) {
    const int n = l & 31, hh = l >> 5, G = l >> 4, tq = (l >> 2) & 3, tp = l & 3;
    __builtin_amdgcn_s_setprio(1);
    dma_chunk(0); dma_chunk(1);
    STAMP(1);
    const u32 xrd = (32 * w + n) * 128, xsw = (n >> 1) & 7;
    const u32 wrd = 16384 + (8 * hh + tq) * 192 + (G & 1) * 32 + tp * 8;
    u32 xa[4];
#pragma unroll
    for (int k = 0; k < 4; ++k) xa[k] = xrd + (((2 * k + hh) ^ xsw) << 4);
    vmwait<7>();
    TIE("+v"(e0));
    const u64 tag = e0 * 6364136223846793005ull + 1442695040888963407ull;
    bar();
    STAMP(2);
    f16v acc[3];
#pragma unroll
    for (int t = 0; t < 3; ++t)
#pragma unroll
      for (int i = 0; i < 4; ++i) {
        const u2 bb = *(AS3 u2*)(size_t)(L_B + t * 64 + i * 16 + hh * 8);
        acc[t][4 * i] = blo(bb[0]); acc[t][4 * i + 1] = bhi(bb[0]); acc[t][4 * i + 2] = blo(bb[1]); acc[t][4 * i + 3] = bhi(bb[1]);
      }
    u4 X = lds128(xa[0]);
    bf8 A[3];
#pragma unroll
    for (int t = 0; t < 3; ++t) A[t] = cat(ldtr(wrd + t * 64), ldtr(wrd + 768 + t * 64));
#pragma unroll
    for (int ks = 0; ks < 32; ++ks) {
      const int c = ks >> 2, k = ks & 3;
      if (k == 3) {
        if (c == 0) vmwait<0>();
        bar();
        if (c == 0) STAMP(13);
        if (c == 1) STAMP(14);
        if (c == 2) STAMP(15);
        if (c == 3) STAMP(3);
        if (c == 5) STAMP(4);
      }
      const int kn = (ks + 1) & 3;
      const u32 sn = (((ks + 1) >> 2) & 3) * SLOT;
      const u4 Xc = X;
      if (ks < 31) X = lds128(sn + xa[kn]);
#pragma unroll
      for (int t = 0; t < 3; ++t) {
        const bf8 Ac = A[t];
        if (ks < 31) A[t] = cat(ldtr(sn + wrd + kn * 3072 + t * 64), ldtr(sn + wrd + kn * 3072 + 768 + t * 64));
        __builtin_amdgcn_sched_barrier(0);
        acc[t] = __builtin_amdgcn_mfma_f32_32x32x16_bf16(Ac, __builtin_bit_cast(bf8, Xc), acc[t], 0, 0, 0);
        __builtin_amdgcn_sched_barrier(0);
      }
    }
    STAMP(5);
    {
      const float c0 = -2.f * 0.7978845608028654f * 1.4426950408889634f, c1 = c0 * 0.044715f;
      const u32 hb = ((4 * R + w) * 86 + 2 * tau0) * 1024 + l * 16;
#pragma unroll
      for (int t = 0; t < 3; ++t) {
        if (t < 2 || three) {
#pragma unroll
          for (int s = 0; s < 2; ++s) {
            u4 o;
#pragma unroll
            for (int i = 0; i < 4; ++i) {
              f2 x = {acc[t][8 * s + 2 * i], acc[t][8 * s + 2 * i + 1]};
              f2 u = x * (x * x * c1 + c0);
              f2 d = {__builtin_amdgcn_exp2f(u[0]), __builtin_amdgcn_exp2f(u[1])};
              d += 1.f;
              f2 r = {__builtin_amdgcn_rcpf(d[0]), __builtin_amdgcn_rcpf(d[1])};
              r *= x;
              o[i] = pk(r[0], r[1]);
            }
            __builtin_amdgcn_raw_buffer_store_b128(o, rws, WS_HF + hb + (2 * t + s) * 1024, 0, 16);
          }
        }
      }
    }
    STAMP(6);
    ev_add(EV_G);
    vmwait<0>();
    STAMP(7);
    if (ev_add(EV_H) == 3) {
      if (l == 0) st_flag(ws + WS_FH + (R * 16 + g) * 8, tag);
      const char* fh = ws + WS_FH + (R * 16 + (l & 15)) * 8;
      poll_from(0, fh, tag);
      ev_add(EV_H);
    }
    ev_wait(EV_H, 5);
    STAMP(8);
    const int tt = w & 1, kh = w >> 1;
    const u32 hsrc = ((2 * rb + tt) * 86 + kh) * 1024 + l * 16;
    constexpr int PF = 10;
    u4 hf[43];
    auto hld = [&](int i) { int p = i + rot2; p = p >= 43 ? p - 43 : p; return __builtin_amdgcn_raw_buffer_load_b128(rws, hsrc, WS_HF + p * 2048, 16); };
#pragma unroll
    for (int i = 0; i < PF; ++i) hf[i] = hld(i);
    const u32 c16 = (G & 1) * 2 + (tp >> 1);
    const u32 ard = kh * 2048 + (4 * hh + tq) * 128 + (tp & 1) * 8;
    const u32 a0 = ard + ((c16 ^ ((tq >> 1) << 2)) << 4), a1 = ard + (((c16 + 4) ^ ((tq >> 1) << 2)) << 4);
    f16v d0, d1;
#pragma unroll
    for (int i = 0; i < 16; ++i) { d0[i] = 0.f; d1[i] = 0.f; }
    ev_wait(EV_W2A, 4);
    STAMP(9);
    bf8 A0 = cat(ldtr(a0), ldtr(a0 + 1024)), A1 = cat(ldtr(a1), ldtr(a1 + 1024));
#pragma unroll
    for (int i = 0; i < 43; ++i) {
      if (i == 27) { STAMP(10); ev_wait(EV_W2B, 4); }
      if (i == 37) { STAMP(11); ev_wait(EV_W2C, 4); }
      if (i + PF < 43) hf[i + PF] = hld(i + PF);
      bf8 N0 = A0, N1 = A1;
      if (i < 42) {
        const u32 ro = (i + 1 < 38 ? i + 1 : i + 1 - 38) * 4096;
        N0 = cat(ldtr(a0 + ro), ldtr(a0 + ro + 1024));
        N1 = cat(ldtr(a1 + ro), ldtr(a1 + ro + 1024));
      }
      __builtin_amdgcn_sched_barrier(0);
      d0 = __builtin_amdgcn_mfma_f32_32x32x16_bf16(A0, __builtin_bit_cast(bf8, hf[i]), d0, 0, 0, 0);
      d1 = __builtin_amdgcn_mfma_f32_32x32x16_bf16(A1, __builtin_bit_cast(bf8, hf[i]), d1, 0, 0, 0);
      if (i == 16) ev_add(EV_M);
      __builtin_amdgcn_sched_barrier(0);
      A0 = N0; A1 = N1;
    }
    STAMP(12);
    const u32 pw = L_P + 2048 + kh * 17408 + (32 * tt + n) * 272 + hh * 16;
#pragma unroll
    for (int i = 0; i < 4; ++i) {
      sts128(pw + i * 32, __builtin_bit_cast(u4, f4{d0[4 * i], d0[4 * i + 1], d0[4 * i + 2], d0[4 * i + 3]}));
      sts128(pw + 128 + i * 32, __builtin_bit_cast(u4, f4{d1[4 * i], d1[4 * i + 1], d1[4 * i + 2], d1[4 * i + 3]}));
    }
    bar();
    DUMP();
  } else {
    const int hw = w4, ht = hw * 64 + l;
    __builtin_amdgcn_s_setprio(2);
    const int ci = hw >> 1, ii = ht & 127, grp = l >> 4, c8 = l & 7, rl = l >> 3;
    const int row0 = 64 * rb + 16 * hw;
    const u32 stg = L_HC + hw * 4096;
#pragma unroll
    for (int j = 0; j < 3; ++j) {
      const int r = row0 - 8 + 8 * j;
      dma16(rl * 1024 + c8 * 16, kk + (r < 0 ? 0 : r) * 1024 + cg * 128, stg + j * 1024);
    }
    dma16((l & 31) * 16, shk + cg * 512, stg + 3072);
    u4 vr[2], hk[3], bv = {0, 0, 0, 0};
#pragma unroll
    for (int j = 0; j < 2; ++j) vr[j] = gld((2 * rl + j) * 1024 + c8 * 16, vv + row0 * 1024 + cg * 128);
    if (hw == 0) {
      const int m = l < 20 ? l : 19;
      const char* bp = m < 12 ? b1 + tau0 * 64 + ((!three && m >= 8) ? m - 8 : m) * 16 : b2 + cg * 128 + (m - 12) * 16;
      asm volatile("global_load_dwordx4 %0, %1, off" : "=v"(bv) : "v"(bp) : "memory");
    }
    hk[0] = gld(ii ? ii * 32 - 16 : 0, ssk + (2 * b + ci) * 4096);
    hk[1] = gld(ii * 32, ssk + (2 * b + ci) * 4096);
    hk[2] = gld(ii * 32 + 16, ssk + (2 * b + ci) * 4096);
    const u32 dd = *(const AS4 u32*)(size_t)(ssd + 4 * b);
    STAMP(13);
    dma_chunk(2);
    if (ht < 16) *(AS3 u32*)(size_t)(L_EV + ht * 4) = 0;
    vmwait<7>();
    TIE("+v"(e0), "+v"(vr[0]), "+v"(vr[1]), "+v"(hk[0]), "+v"(hk[1]), "+v"(hk[2]), "+v"(bv));
    const u64 tag = e0 * 6364136223846793005ull + 1442695040888963407ull;
    if (ht < 20) sts128(L_B + ht * 16, bv);
    if (row0 == 0 && l < 24) sts128(stg + 640 + l * 16, u4{0, 0, 0, 0});
    u4 kr[5], sk[4], qr[2], xv[2], gv[2];
#pragma unroll
    for (int j = 0; j < 5; ++j) kr[j] = lds128(stg + (5 + 2 * rl + j) * 128 + c8 * 16);
#pragma unroll
    for (int j = 0; j < 4; ++j) sk[j] = lds128(stg + 3072 + c8 * 64 + j * 16);
    float gacc = 0.f;
    const u32 gx = ((rb & 1) * 64 + l) * 128, gs = (l >> 1) & 7;
    auto gate_ld = [&](int c) {
#pragma unroll
      for (int e = 0; e < 2; ++e) {
        xv[e] = lds128((c & 3) * SLOT + gx + (((2 * hw + e) ^ gs) << 4));
        gv[e] = *(const AS4 u4*)(size_t)(gw + ((c + rot1) & 7) * 128 + hw * 32 + e * 16);
      }
      __builtin_amdgcn_sched_barrier(0);
    };
    auto gate = [&]() {
#pragma unroll
      for (int e = 0; e < 2; ++e)
#pragma unroll
        for (int i = 0; i < 4; ++i) gacc = dot2(gv[e][i], xv[e][i], gacc);
      TIE("+v"(gacc));
    };
    auto q_ld = [&]() {
#pragma unroll
      for (int e = 0; e < 2; ++e)
        qr[e] = lds128(((rb & 1) * 64 + 16 * hw + (l & 15)) * 128 + (((grp + 4 * e) ^ ((l & 15) >> 1)) << 4));
    };
    STAMP(1);
    bar();
    STAMP(2);
    gate_ld(0);
    q_ld();
#pragma unroll
    for (int e = 0; e < 8; ++e) {
      if (e < 7) dma_piece(3, e);
      const u32 sel = (e & 1) ? 0x07060302u : 0x05040100u;
      const int m = e >> 1;
      const u32 sa = sk[m][(e & 1) * 2], sb = sk[m][(e & 1) * 2 + 1];
      float r0 = dot2(__builtin_amdgcn_perm(kr[0][m], kr[1][m], sel), sb, 0.f);
      float r1 = dot2(__builtin_amdgcn_perm(kr[1][m], kr[2][m], sel), sb, 0.f);
      r0 = dot2(__builtin_amdgcn_perm(kr[2][m], kr[3][m], sel), sa, r0);
      r1 = dot2(__builtin_amdgcn_perm(kr[3][m], kr[4][m], sel), sa, r1);
      r0 *= (e & 1) ? bhi(vr[0][m]) : blo(vr[0][m]);
      r1 *= (e & 1) ? bhi(vr[1][m]) : blo(vr[1][m]);
      *(AS3 u32*)(size_t)(L_U + c8 * 1056 + e * 128 + (8 * hw + rl) * 4) = pk(r0, r1);
      __builtin_amdgcn_sched_barrier(0);
    }
    gate();
    STAMP(14);
    bar();
    gate_ld(1);
#pragma unroll
    for (int e = 0; e < 2; ++e) {
      const int ch = 16 * hw + 8 * e + rl;
      u4 v = lds128(L_U + (ch >> 3) * 1056 + (ch & 7) * 128 + c8 * 16);
      __builtin_amdgcn_raw_buffer_store_b128(v, rws, WS_KVT + (64 * cg + ch) * 4096 + rb * 128 + c8 * 16, 0, 16);
    }
    {
      u32 W[13];
#pragma unroll
      for (int j = 0; j < 12; ++j) W[j] = (j < 4 && ii == 0) ? 0 : hk[j >> 2][j & 3];
      W[12] = 0;
      auto A = [&](int j) { return __builtin_amdgcn_alignbit(W[j + 1], W[j], 16); };
      const u32 p = L_HC + ci * 16896 + 64 + ii * 32;
      dma_piece(4, 0);
      sts128(p, u4{W[4], W[5], W[6], W[7]});                 sts128(p + 16, u4{W[8], W[9], W[10], W[11]});
      dma_piece(4, 1);
      sts128(p + 4224, u4{W[3], W[4], W[5], W[6]});          sts128(p + 4240, u4{W[7], W[8], W[9], W[10]});
      dma_piece(4, 2);
      sts128(p + 8448, u4{A(1), A(2), A(3), A(4)});          sts128(p + 8464, u4{A(5), A(6), A(7), A(8)});
      dma_piece(4, 3);
      sts128(p + 12672, u4{A(0), A(1), A(2), A(3)});         sts128(p + 12688, u4{A(4), A(5), A(6), A(7)});
      dma_piece(4, 4);
      if (ii == 127) {
        sts128(p + 32, u4{0, 0, 0, 0});                      sts128(p + 4224 + 32, u4{W[11], 0, 0, 0});
        sts128(p + 8448 + 32, u4{A(9), A(10), A(11), 0});    sts128(p + 12672 + 32, u4{A(8), A(9), A(10), A(11)});
      }
      dma_piece(4, 5);
      if (ii < 16) sts128(L_HC + ci * 16896 + (ii >> 2) * 4224 + (ii & 3) * 16, u4{0, 0, 0, 0});
      dma_piece(4, 6);
    }
    gate();
    const u32 w2o = rl * 1024 + ((c8 ^ (((l >> 4) & 1) << 2)) << 4);
    auto w2p = [&](int j) { int p = j + 4 * rot2; p = p >= 172 ? p - 172 : p; return w2 + cg * 128 + p * 8192; };
    auto dma_w2 = [&](int s) {
#pragma unroll
      for (int j = 0; j < 7; ++j) dma16(w2o, w2p(28 * s + 7 * hw + j), (28 * s + 7 * hw + j) * 1024);
    };
    vmwait<16>();
    STAMP(15);
    bar();
    gate_ld(2); dma_chunk(5); gate();
    vmwait<14>();
    if (ev_add(EV_KV) == 3 && l == 0) st_flag(ws + WS_FKV + (cg * 32 + rb) * 8, tag);
    STAMP(3);
    bar();
    gate_ld(3); dma_chunk(6); gate();
    vmwait<14>();
    bar();
    if (R & 1) q_ld();
    gate_ld(4); dma_chunk(7); gate();
    vmwait<14>();
    bar();
    gate_ld(5); dma_w2(0); gate();
    vmwait<14>();
    bar();
    gate_ld(6); dma_w2(1); gate();
    vmwait<14>();
    bar();
    u64 fv = gld8_sc1((l & 31) * 8, ws + WS_FKV + cg * 256);
    gate_ld(7); dma_w2(2); gate();
    *(AS3 float*)(size_t)(L_GW + ht * 4) = gacc;
    bar();
    STAMP(4);
    vmwait<7>();
    TIE("+v"(fv));
    poll_from(fv, ws + WS_FKV + cg * 256 + (l & 31) * 8, tag);
    {
      u4 uu[2];
#pragma unroll
      for (int e = 0; e < 2; ++e) uu[e] = gld_sc1(((hw & 1) * 128 + e * 64 + l) * 16, ws + WS_KVT + (2 * b + ci) * 4096);
      dma_w2(3);
      if (hw < 2) sts128(L_U + (hw * 4 + grp) * 1280 + (l & 15) * 16, u4{0, 0, 0, 0});
      vmwait<7>();
      TIE("+v"(uu[0]), "+v"(uu[1]));
#pragma unroll
      for (int e = 0; e < 2; ++e) {
        const int p = (hw & 1) * 128 + e * 64 + l;
        sts128(L_U + ci * 5120 + (p & 3) * 1280 + ((p >> 2) + 16) * 16, uu[e]);
      }
    }
    ev_add(EV_U); ev_wait(EV_U, 4);
    __builtin_amdgcn_s_setprio(0);
    u4 wr[15];
    {
      const int hf = hw & 1, G = l >> 4, tq = (l >> 2) & 3, tp = l & 3;
      const u32 ab = L_HC + ci * 16896 + (tq & 1) * 8448 + (tq >> 1) * 4224 + (8 * tp - 8 * G + 28 + (tq & 1) * 4 + 256 * hf) * 2;
      const u32 ub = L_U + ci * 5120 + G * 1280 + ((l & 15) + 9 - 8 * hf) * 16;
      f4 y[4][2];
#pragma unroll
      for (int j = 0; j < 4; ++j) { y[j][0] = f4{0, 0, 0, 0}; y[j][1] = f4{0, 0, 0, 0}; }
      u4 Bn[4]; bf4 An[12];
      auto rd = [&](int st, int m) {
        if (m < 3) An[m] = ldtr(ab + 64 * st + 8 * m);
        else if (m < 7) Bn[m - 3] = lds128(ub + (16 * (m - 3) + 7 - st) * 16);
        else An[m - 4] = ldtr(ab + 1024 * ((m - 4) / 3) + 64 * st + 8 * ((m - 4) % 3));
      };
#pragma unroll
      for (int m = 0; m < 16; ++m) rd(0, m);
      ev_wait(EV_G, 3);
      STAMP(5);
#pragma unroll
      for (int st = 0; st < 8; ++st) {
        u4 B[4]; bf4 A[12];
#pragma unroll
        for (int j = 0; j < 4; ++j) B[j] = Bn[j];
#pragma unroll
        for (int j = 0; j < 12; ++j) A[j] = An[j];
        int m = 0;
#pragma unroll
        for (int i = 0; i < 4; ++i) {
#pragma unroll
          for (int j = 0; j + i < 4; ++j)
#pragma unroll
            for (int r = 0; r < 2; ++r) {
              __builtin_amdgcn_sched_barrier(0);
              y[j + i][r] = __builtin_amdgcn_mfma_f32_16x16x32_bf16(cat(A[3 * i + 1 + r], A[3 * i + r]),
                                                                   __builtin_bit_cast(bf8, B[j]), y[j + i][r], 0, 0, 0);
              __builtin_amdgcn_sched_barrier(0);
              if (st < 7 && m < 16) rd(st + 1, m);
              if (st < 5 && m % 6 == 3) { const int k = 3 * st + m / 6; wr[k] = gld(w2o, w2p(k < 10 ? 112 + 10 * hw + k : 142 + 5 * hw + k)); }
              ++m;
            }
        }
        if (st == 1) { vmwait<6>(); ev_add(EV_W2A); }
      }
      __builtin_amdgcn_s_setprio(2);
      STAMP(6);
      const u32 xa = L_HC + ci * 16896 + G * 4224 + 128 + (l & 15) * 16;
      f4 own[4];
#pragma unroll
      for (int m = 0; m < 4; ++m) {
        own[m] = hf ? y[2 + (m >> 1)][m & 1] : y[m >> 1][m & 1];
        sts128(xa + 1024 * m + 512 * hf, __builtin_bit_cast(u4, hf ? y[m >> 1][m & 1] : y[2 + (m >> 1)][m & 1]));
      }
      ev_add(EV_PA + ci); ev_wait(EV_PA + ci, 2);
      u4 pr[4], u[2];
#pragma unroll
      for (int m = 0; m < 4; ++m) pr[m] = lds128(xa + 1024 * m + 512 * (1 - hf));
#pragma unroll
      for (int e = 0; e < 2; ++e) u[e] = lds128(L_U + ci * 5120 + G * 1280 + (16 * (2 * hf + e) + (l & 15) + 16) * 16);
      ev_add(EV_CD);
      const float D = ci ? bhi(dd) : blo(dd);
      const int src = ((l >> 2) + 16 * (l & 3)) * 4;
#pragma unroll
      for (int e = 0; e < 2; ++e) {
        u4 zo;
#pragma unroll
        for (int r = 0; r < 2; ++r) {
          const f4 s = own[2 * e + r] + __builtin_bit_cast(f4, pr[2 * e + r]);
          zo[2 * r] = __builtin_amdgcn_ds_bpermute(src, pk(s[0] + D * blo(u[e][2 * r]), s[1] + D * bhi(u[e][2 * r])));
          zo[2 * r + 1] = __builtin_amdgcn_ds_bpermute(src, pk(s[2] + D * blo(u[e][2 * r + 1]), s[3] + D * bhi(u[e][2 * r + 1])));
        }
        __builtin_amdgcn_raw_buffer_store_b128(zo, rws, WS_YT + (2 * b + ci) * 4096 + (2 * hf + e) * 1024 + l * 16, 0, 16);
      }
    }
    vmwait<2>();
    TIE("+v"(wr[0]), "+v"(wr[1]), "+v"(wr[2]), "+v"(wr[3]), "+v"(wr[4]), "+v"(wr[5]), "+v"(wr[6]), "+v"(wr[7]));
    TIE("+v"(wr[8]), "+v"(wr[9]), "+v"(wr[10]), "+v"(wr[11]), "+v"(wr[12]), "+v"(wr[13]), "+v"(wr[14]));
    ev_wait(EV_CD, 4);
#pragma unroll
    for (int j = 0; j < 10; ++j) sts128((112 + 10 * hw + j) * 1024 + l * 16, wr[j]);
    ev_add(EV_W2B);
    STAMP(7);
    vmwait<0>();
    if (ev_add(EV_Y) == 3 && l == 0) st_flag(ws + WS_FY + b * 8, tag);
    STAMP(8);
    ev_wait(EV_M, 4);
#pragma unroll
    for (int j = 0; j < 5; ++j) sts128((5 * hw + j) * 1024 + l * 16, wr[10 + j]);
    ev_add(EV_W2C);
    const char* fy = ws + WS_FY + (32 * cg + 8 * hw + (l & 7)) * 8;
    poll_from(ld_flag(fy), fy, tag);
    STAMP(9);
#pragma unroll
    for (int e = 0; e < 2; ++e) {
      const int ch = 16 * hw + 8 * e + (l >> 3);
      u4 v = __builtin_amdgcn_raw_buffer_load_b128(rws, (64 * cg + ch) * 4096 + rb * 128 + (l & 7) * 16, WS_YT, 16);
      sts128(L_Z + 2048 + ch * 144 + (l & 7) * 16, v);
    }
    if (b == 0 && hw == 0) {
#pragma unroll
      for (int j = 0; j < 4; ++j) {
        const char* p = ws + WS_FKV + (j * 64 + l) * 8;
        poll_from(ld_flag(p), p, tag);
      }
      if (l == 0) st_flag(ws, tag);
    }
    ev_add(EV_Z); ev_wait(EV_Z, 4);
    float gl = 0.f;
#pragma unroll
    for (int j = 0; j < 4; ++j) gl += *(AS3 float*)(size_t)(L_GW + (64 * j + 16 * hw + (l & 15)) * 4);
    const float gt = __builtin_amdgcn_rcpf(1.f + __builtin_amdgcn_exp2f(-1.4426950408889634f * gl));
    float ap[2][8];
#pragma unroll
    for (int e = 0; e < 2; ++e) {
      const u32 a = L_Z + 2048 + (8 * (grp + 4 * e) + ((l >> 2) & 3)) * 144 + (16 * hw + 4 * (l & 3)) * 2;
      u4 z = __builtin_bit_cast(u4, cat(ldtr(a), ldtr(a + 4 * 144)));
      const u4 br = lds128(L_B + 192 + (grp + 4 * e) * 16);
#pragma unroll
      for (int i = 0; i < 4; ++i) {
        ap[e][2 * i] = gt * blo(qr[e][i]) * blo(z[i]) + (1.f - gt) * blo(br[i]);
        ap[e][2 * i + 1] = gt * bhi(qr[e][i]) * bhi(z[i]) + (1.f - gt) * bhi(br[i]);
      }
    }
    STAMP(10);
    bar();
    STAMP(11);
#pragma unroll
    for (int e = 0; e < 2; ++e) {
      const u32 a = L_P + 2048 + (16 * hw + (l & 15)) * 272 + (grp + 4 * e) * 32;
      u4 o;
#pragma unroll
      for (int r = 0; r < 2; ++r) {
        f4 s = __builtin_bit_cast(f4, lds128(a + r * 16)) + __builtin_bit_cast(f4, lds128(a + 17408 + r * 16));
        o[2 * r] = pk(ap[e][4 * r] + (1.f - gt) * s[0], ap[e][4 * r + 1] + (1.f - gt) * s[1]);
        o[2 * r + 1] = pk(ap[e][4 * r + 2] + (1.f - gt) * s[2], ap[e][4 * r + 3] + (1.f - gt) * s[3]);
      }
      *(u4*)(out + (row0 + (l & 15)) * 1024 + cg * 128 + (grp + 4 * e) * 16) = o;
    }
    STAMP(12);
    DUMP();
  }
}

extern "C" void kernel_launch(void* const* d_in, const int* in_sizes, int n_in, void* d_out, int out_size, void* d_ws,
                              size_t ws_size, hipStream_t stream) {
  (void)in_sizes; (void)n_in; (void)out_size; (void)ws_size;
  (void)hipFuncSetAttribute((const void*)fused_kernel, hipFuncAttributeMaxDynamicSharedMemorySize, LDS_BYTES);
  fused_kernel<<<256, 512, LDS_BYTES, stream>>>(
      (const char*)d_in[0], (const char*)d_in[1], (const char*)d_in[2], (const char*)d_in[3], (const char*)d_in[4],
      (const char*)d_in[5], (const char*)d_in[7], (const char*)d_in[8], (const char*)d_in[9], (const char*)d_in[10],
      (const char*)d_in[11], (char*)d_out, (char*)d_ws);
}
```

```cpp
#include <hip/hip_runtime.h>
#include <hip/hip_bf16.h>

typedef unsigned u32;
typedef unsigned long long u64;
typedef unsigned u4 __attribute__((ext_vector_type(4)));
typedef unsigned u2 __attribute__((ext_vector_type(2)));
typedef float f2 __attribute__((ext_vector_type(2)));
typedef float f4 __attribute__((ext_vector_type(4)));
typedef float f16v __attribute__((ext_vector_type(16)));
typedef __bf16 bf2 __attribute__((ext_vector_type(2)));
typedef __bf16 bf4 __attribute__((ext_vector_type(4)));
typedef __bf16 bf8 __attribute__((ext_vector_type(8)));
#define AS3 __attribute__((address_space(3)))
#define AS1 __attribute__((address_space(1)))
#define AS4 __attribute__((address_space(4)))
#define DEV __device__ __forceinline__

constexpr u32 WS_FKV = 4096, WS_FH = 8192;
constexpr u32 WS_KVT = 1u << 20, WS_YT = 4u << 20, WS_HF = 8u << 20, WS_TS = 16u << 20;
constexpr u32 SLOT = 28672, L_HC = 114688, L_U = L_HC + 33792, L_MISC = L_U + 10240;
constexpr u32 L_GW = L_MISC, L_B = L_MISC + 1024, L_EV = L_MISC + 2048, L_GV = L_MISC + 3072, L_B1 = L_MISC + 4096;
constexpr u32 L_Z = 18432, L_P = 28672, LDS_BYTES = 163840;
enum { EV_KV = 0, EV_H, EV_W2A, EV_PA, EV_PB, EV_U, EV_Y, EV_CD, EV_M, EV_W2B, EV_W2C, EV_Z, EV_G };
#define PSLEEP 8
#define PFN 10
#define DA 2
#define HB 1
#define LP 1
#define CPRIO 2
#define WA 0
#define GP 3
#define GN 3
#define PW2 6
#define PI 2
#define HS 0
#define WRP 0
#define ZSL 4
#define PC1 1
#define OAUX 0
constexpr int SPIN_MAX = 1 << 18;

DEV void dma16(u32 voff, const char* sb, u32 lds) {
  u32 keep;
  asm volatile("s_mov_b32 %0, m0\n\ts_mov_b32 m0, %3\n\ts_nop 0\n\tglobal_load_lds_dwordx4 %1, %2\n\ts_mov_b32 m0, %0"
               : "=&s"(keep) : "v"(voff), "s"(sb), "s"(lds) : "memory");
}
DEV u4 gld(u32 voff, const char* sb) {
  u4 r; asm volatile("global_load_dwordx4 %0, %1, %2" : "=v"(r) : "v"(voff), "s"(sb) : "memory"); return r;
}
DEV u4 gld_sc1(u32 voff, const char* sb) {
  u4 r; asm volatile("global_load_dwordx4 %0, %1, %2 sc1" : "=v"(r) : "v"(voff), "s"(sb) : "memory"); return r;
}
DEV u64 gld8_sc1(u32 voff, const char* sb) {
  u64 r; asm volatile("global_load_dwordx2 %0, %1, %2 sc1" : "=v"(r) : "v"(voff), "s"(sb) : "memory"); return r;
}
DEV u32 touch(u32 voff, const char* sb) {
  u32 r; asm volatile("global_load_dword %0, %1, %2" : "=v"(r) : "v"(voff), "s"(sb) : "memory"); return r;
}
DEV u32 fkv(int cg, int rb) { return WS_FKV + (cg * 32 + rb) * 8; }
DEV u32 fhh(int r32, int g) { return WS_FH + (r32 * 16 + g) * 8; }
#define TIE(...) asm volatile("" : __VA_ARGS__)
DEV float dot2(u32 a, u32 b, float c) {
  return __builtin_amdgcn_fdot2_f32_bf16(__builtin_bit_cast(bf2, a), __builtin_bit_cast(bf2, b), c, false);
}
template <int N> DEV void vmwait() { asm volatile("s_waitcnt vmcnt(%0)" ::"n"(N) : "memory"); }
DEV void bar() { asm volatile("s_waitcnt lgkmcnt(0)\n\ts_barrier" ::: "memory"); }
DEV u4 lds128(u32 a) { return *(AS3 u4*)(size_t)a; }
DEV void sts128(u32 a, u4 v) { *(AS3 u4*)(size_t)a = v; }
DEV bf4 ldtr(u32 a) { return __builtin_amdgcn_ds_read_tr16_b64_v4bf16((AS3 bf4*)(size_t)a); }
DEV bf8 cat(bf4 a, bf4 b) { return __builtin_shufflevector(a, b, 0, 1, 2, 3, 4, 5, 6, 7); }
DEV float blo(u32 v) { return __builtin_bit_cast(float, v << 16); }
DEV float bhi(u32 v) { return __builtin_bit_cast(float, v & 0xffff0000u); }
DEV u32 pk(float a, float b) { f2 t = {a, b}; return __builtin_bit_cast(u32, __builtin_convertvector(t, bf2)); }
#define NOPK(x)
DEV u32 ev_add(int e) {
  u32 old = 0;
  asm volatile("s_waitcnt lgkmcnt(0)" ::: "memory");
  if ((threadIdx.x & 63) == 0)
    old = __hip_atomic_fetch_add((AS3 u32*)(size_t)(L_EV + 4 * e), 1u, __ATOMIC_RELAXED, __HIP_MEMORY_SCOPE_WORKGROUP);
  old = __builtin_amdgcn_readfirstlane(old);
  asm volatile("" ::: "memory");
  return old;
}
DEV void ev_post(int e) {
  asm volatile("" ::: "memory");
  if ((threadIdx.x & 63) == 0)
    __hip_atomic_fetch_add((AS3 u32*)(size_t)(L_EV + 4 * e), 1u, __ATOMIC_RELAXED, __HIP_MEMORY_SCOPE_WORKGROUP);
  asm volatile("" ::: "memory");
}
DEV u32 ev_peek(int e) { return *(volatile AS3 u32*)(size_t)(L_EV + 4 * e); }
DEV void ev_wait(int e, u32 n) {
  asm volatile("" ::: "memory");
#pragma unroll 1
  for (int it = 0; it < SPIN_MAX; ++it) {
    u32 v = *(volatile AS3 u32*)(size_t)(L_EV + 4 * e);
    if (__builtin_amdgcn_readfirstlane(v) >= n) break;
    __builtin_amdgcn_s_sleep(1);
  }
  asm volatile("" ::: "memory");
}
DEV u64 ld_flag(const char* p) {
  u64 r; asm volatile("global_load_dwordx2 %0, %1, off sc1\n\ts_waitcnt vmcnt(0)" : "=&v"(r) : "v"(p) : "memory"); return r;
}
DEV void st_flag(char* p, u64 v) { __hip_atomic_store((AS1 u64*)p, v, __ATOMIC_RELAXED, __HIP_MEMORY_SCOPE_AGENT); }
DEV void poll_from(u64 v, const char* p, u64 tag) {
#pragma unroll 1
  for (int it = 0; it < SPIN_MAX; ++it) {
    if (__builtin_amdgcn_ballot_w64(v != tag) == 0) break;
    __builtin_amdgcn_s_sleep(PSLEEP);
    v = ld_flag(p);
  }
  asm volatile("" ::: "memory");
}
struct Args { const char *q, *kk, *vv, *shk, *ssk, *ssd, *w1, *b1, *w2, *b2, *gw; char *out, *ws; };
__global__ __launch_bounds__(512, 2) void fused_kernel(Args) {
  if (PI) {
    const char* pc0;
    asm volatile("s_getpc_b64 %0" : "=s"(pc0));
    const u32 o = ((((blockIdx.x >> 3) & (32 / PI - 1)) * 8 + (threadIdx.x >> 6)) * PI * 8 + (threadIdx.x & (PI * 8 - 1))) * 16;
    dma16(min(o, 31232u), (const char*)((size_t)pc0 & ~(size_t)127), L_GW);
  }
  const AS4 Args* ka = (const AS4 Args*)__builtin_amdgcn_kernarg_segment_ptr();
  asm volatile("" : "+s"(ka));
  const char *q = ka->q, *kk = ka->kk, *vv = ka->vv, *shk = ka->shk, *ssk = ka->ssk, *ssd = ka->ssd, *w1 = ka->w1, *b1 = ka->b1,
             *w2 = ka->w2, *b2 = ka->b2, *gw = ka->gw;
  char *out = ka->out, *ws = ka->ws;
  asm volatile("" ::"s"(q), "s"(kk), "s"(vv), "s"(shk), "s"(ssk), "s"(ssd), "s"(w1), "s"(b1), "s"(w2), "s"(b2), "s"(gw), "s"(out), "s"(ws));
  const int bi = blockIdx.x, l = threadIdx.x & 63;
  const int w = __builtin_amdgcn_readfirstlane(threadIdx.x >> 6);
  const int par = bi & 1, rb = ((bi & 7) >> 1) * 8 + ((bi >> 3) & 7), cg = 4 * par + (bi >> 6);
  const int R = rb >> 1, gl = (rb & 1) * 4 + (bi >> 6), g = 8 * par + gl;
  const bool three = gl < 6 - par;
  const int tau0 = 22 * par + (three ? 3 * gl : 2 * gl + 6 - par);
  const int px = bi >> 3;
  const int b = 32 * cg + rb;
  const int w4 = w & 3;
  const __amdgpu_buffer_rsrc_t rws = __builtin_amdgcn_make_buffer_rsrc(ws, 0, 32u << 20, 0x00020000);
  const __amdgpu_buffer_rsrc_t rout = __builtin_amdgcn_make_buffer_rsrc(out, 0, 2u << 20, 0x00020000);
  u32 xo[2], wo[3];
#pragma unroll
  for (int p = 0; p < 2; ++p) xo[p] = (l >> 3) * 1024 + (((l & 7) ^ (4 * p + (l >> 4))) << 4);
#pragma unroll
  for (int j = 0; j < 3; ++j) {
    int n = (3 * w4 + j) * 64 + l, row = n / 12, c = n % 12;
    if (!three && c >= 8) c -= 8;
    wo[j] = row * 2752 + c * 16;
  }
  const char* xb = q + (size_t)(R * 128 + w4 * 32) * 1024;
  const char* w1b = w1 + tau0 * 64;
  const int qh = rb & 1, rot1 = (cg + 4 * qh) & 7;
  auto dma_piece = [&](int c, int j) {
    const u32 s = (c & 3) * SLOT;
    const int pc = (c + rot1) & 7;
    if (j < 4) dma16(xo[j & 1], xb + j * 8192 + pc * 128, s + (4 * w4 + j) * 1024);
    else dma16(wo[j - 4], w1b + pc * (64 * 2752), s + 16384 + (3 * w4 + j - 4) * 1024);
  };
  auto dma_chunk = [&](int c) {
#pragma unroll
    for (int j = 0; j < 7; ++j) dma_piece(c, j);
  };
  const u32 w2o = (l >> 3) * 1024 + (((l & 7) ^ (((l >> 4) & 1) << 2)) << 4);
  auto w2p = [&](int j) { return w2 + cg * 128 + (j ^ (j < 128 ? 4 * px : j < 160 ? px & ~3 : 0)) * 8192; };

  if (w < 4) {
    const int n = l & 31, hh = l >> 5, G = l >> 4, tq = (l >> 2) & 3, tp = l & 3;
    __builtin_amdgcn_s_setprio(1);
    u64 e0 = gld8_sc1(0, ws);
    dma16((l < (three ? 12 : 8) ? l : 0) * 16, b1 + tau0 * 64, L_B1);
    dma_chunk(0); dma_chunk(1); dma_chunk(2);
    vmwait<21>();
    f16v acc[3];
#pragma unroll
    for (int t = 0; t < 3; ++t)
#pragma unroll
      for (int i = 0; i < 4; ++i) {
        const u2 bb = *(AS3 u2*)(size_t)(L_B1 + t * 64 + i * 16 + hh * 8);
        acc[t][4 * i] = blo(bb[0]); acc[t][4 * i + 1] = bhi(bb[0]); acc[t][4 * i + 2] = blo(bb[1]); acc[t][4 * i + 3] = bhi(bb[1]);
      }
    TIE("+v"(acc[0]), "+v"(acc[1]), "+v"(acc[2]));
    dma_chunk(3);
    __builtin_amdgcn_s_setprio(PC1);
    const u32 xrd = (32 * w + n) * 128, xsw = (n >> 1) & 7;
    const u32 wrd = 16384 + (8 * hh + tq) * 192 + (G & 1) * 32 + tp * 8;
    u32 xa[4];
#pragma unroll
    for (int k = 0; k < 4; ++k) xa[k] = xrd + (((2 * k + hh) ^ xsw) << 4);
    u32 xh[4] = {xa[0] + 2 * SLOT, xa[1] + 2 * SLOT, xa[2] + 2 * SLOT, xa[3] + 2 * SLOT}, wrh = wrd + 2 * SLOT;
    TIE("+v"(xh[0]), "+v"(xh[1]), "+v"(xh[2]), "+v"(xh[3]), "+v"(wrh));
    vmwait<21>();
    TIE("+v"(e0));
    const u64 tag = e0 * 6364136223846793005ull + 1442695040888963407ull;
    bar();
    u32 tc = 0, td = 0;
    u4 X[32];
    bf8 A[32][3];
    auto rd = [&](int ks, int m) {
      const u32 sn = ((ks >> 2) & 1) * SLOT;
      const int kn = ks & 3, hi = (ks >> 3) & 1;
      if (ks >= 32) return;
      const u32 wa = (hi ? wrh : wrd) + sn + kn * 3072 + m * 64;
      if (m == 3) X[ks] = lds128(sn + (hi ? xh[kn] : xa[kn]));
      else A[ks][m] = cat(ldtr(wa), ldtr(wa + 768));
    };
    rd(0, 3); rd(0, 0); rd(0, 1); rd(0, 2); rd(1, 3); rd(1, 0); rd(1, 1); rd(1, 2);
#pragma unroll
    for (int ks = 0; ks < 32; ++ks) {
      const int c = ks >> 2, k = ks & 3;
#pragma unroll
      for (int t = 0; t < 3; ++t) {
        __builtin_amdgcn_sched_barrier(0);
        acc[t] = __builtin_amdgcn_mfma_f32_32x32x16_bf16(A[ks][t], __builtin_bit_cast(bf8, X[ks]), acc[t], 0, 0, 0);
        __builtin_amdgcn_sched_barrier(0);
        if (k != 2) {
          if (t == 0) rd(ks + 2, 3);
          rd(ks + 2, t);
        } else if (t == 1) {
          if (c == 0) vmwait<14>();
          if (c == 1) vmwait<7>();
          if (c == 2) vmwait<0>();
          bar();
          if (c == 3 && w == 3 && l < 1) st_flag(ws + fkv(cg, rb), tag);
          rd(ks + 2, 3); rd(ks + 2, 0);
        } else if (t == 2) {
          rd(ks + 2, 1); rd(ks + 2, 2);
        }
      }
    }
    {
      const float c0 = -2.f * 0.7978845608028654f * 1.4426950408889634f, c1 = c0 * 0.044715f;
      const u32 hb = ((4 * R + w) * 86 + 2 * tau0) * 1024 + l * 16;
#pragma unroll
      for (int t = 0; t < 3; ++t) {
        if (t < 2 || three) {
#pragma unroll
          for (int s = 0; s < 2; ++s) {
            u4 o;
#pragma unroll
            for (int i = 0; i < 4; ++i) {
              f2 x = {acc[t][8 * s + 2 * i], acc[t][8 * s + 2 * i + 1]};
              f2 u = x * (x * x * c1 + c0);
              f2 d = {__builtin_amdgcn_exp2f(u[0]), __builtin_amdgcn_exp2f(u[1])};
              d += 1.f;
              f2 r = {__builtin_amdgcn_rcpf(d[0]), __builtin_amdgcn_rcpf(d[1])};
              r *= x;
              o[i] = pk(r[0], r[1]);
            }
            __builtin_amdgcn_raw_buffer_store_b128(o, rws, WS_HF + hb + (2 * t + s) * 1024, 0, 16);
          }
        }
      }
    }
    ev_post(EV_G);
    __builtin_amdgcn_s_setprio(1);
    auto dma_w2 = [&](int j) { dma16(w2o, w2p(4 * j + w), (4 * j + w) * 1024); };
#pragma unroll
    for (int j = 0; j < WA; ++j) dma_w2(j);
    if (b == 0 && w == 0) {
      u64 f[4];
#pragma unroll
      for (int j = 0; j < 4; ++j) f[j] = gld8_sc1(fkv(2 * j + (l >> 5), l & 31), ws);
      vmwait<0>();
      TIE("+v"(f[0]), "+v"(f[1]), "+v"(f[2]), "+v"(f[3]));
#pragma unroll
      for (int j = 0; j < 4; ++j) poll_from(f[j], ws + fkv(2 * j + (l >> 5), l & 31), tag);
    }
    vmwait<WA>();
    TIE("+v"(tc), "+v"(td));
    if (l == 0) st_flag(ws + fhh(4 * R + w, g), tag);
    if (b == 0 && threadIdx.x == 0) st_flag(ws, tag);
#pragma unroll
    for (int j = HS ? 7 * HS : WA; j < 28; ++j) dma_w2(j);
    const char* fh = ws + fhh(2 * rb + (w & 1), l & 15);
    poll_from(ld_flag(fh), fh, tag);
    vmwait<0>();
    ev_post(EV_W2A);
    const int tt = w & 1, kh = w >> 1;
    const u32 hsrc = ((2 * rb + tt) * 86 + kh) * 1024 + l * 16;
    constexpr int PF = PFN;
    u4 hf[43];
    u32 hx0 = px * 2048, hx1 = (px >> 2) * 2048;
    TIE("+s"(hx0), "+s"(hx1));
    auto hld = [&](int i) { return __builtin_amdgcn_raw_buffer_load_b128(rws, hsrc, (WS_HF + i * 2048) ^ (i < 32 ? hx0 : i < 40 ? hx1 : 0), 16); };
#pragma unroll
    for (int i = 0; i < PF; ++i) hf[i] = hld(i);
    ev_wait(EV_W2A, HS ? 8 : 4);
    const u32 c16 = (G & 1) * 2 + (tp >> 1);
    const u32 ard = kh * 2048 + (4 * hh + tq) * 128 + (tp & 1) * 8;
    const u32 a0 = ard + ((c16 ^ ((tq >> 1) << 2)) << 4), a1 = ard + (((c16 + 4) ^ ((tq >> 1) << 2)) << 4);
    f16v d0, d1;
#pragma unroll
    for (int i = 0; i < 16; ++i) { d0[i] = 0.f; d1[i] = 0.f; }
    bf8 A0[43], A1[43];
    u32 ab0[3] = {a0, a0 + 61440, a0 + 122880}, ab1[3] = {a1, a1 + 61440, a1 + 122880};
    TIE("+v"(ab0[1]), "+v"(ab0[2]), "+v"(ab1[1]), "+v"(ab1[2]));
    auto ald = [&](int j, int h) {
      if (j >= 43) return;
      const int r = j < 38 ? j : j - 38;
      const u32 a = (h ? ab1[r / 15] : ab0[r / 15]) + (r % 15) * 4096;
      (h ? A1[j] : A0[j]) = cat(ldtr(a), ldtr(a + 1024));
    };
#pragma unroll
    for (int j = 0; j < DA; ++j) { ald(j, 0); ald(j, 1); }
    u32 pe = 0;
#pragma unroll
    for (int i = 0; i < 43; ++i) {
      if (i + DA == 28) { if (__builtin_amdgcn_readfirstlane(pe) < 4) ev_wait(EV_W2B, 4); }
      if (i + DA == 38) { if (__builtin_amdgcn_readfirstlane(pe) < 4) ev_wait(EV_W2C, 4); }
      if (i + DA == 26) pe = ev_peek(EV_W2B);
      if (i + DA == 36) pe = ev_peek(EV_W2C);
      __builtin_amdgcn_sched_barrier(0);
      d0 = __builtin_amdgcn_mfma_f32_32x32x16_bf16(A0[i], __builtin_bit_cast(bf8, hf[i]), d0, 0, 0, 0);
      __builtin_amdgcn_sched_barrier(0);
      ald(i + DA, 0);
      if (LP == 0) ald(i + DA, 1);
      __builtin_amdgcn_sched_barrier(0);
      d1 = __builtin_amdgcn_mfma_f32_32x32x16_bf16(A1[i], __builtin_bit_cast(bf8, hf[i]), d1, 0, 0, 0);
      __builtin_amdgcn_sched_barrier(0);
      if (LP == 1) ald(i + DA, 1);
      __builtin_amdgcn_sched_barrier(0);
      if (i % HB == 0) {
#pragma unroll
        for (int j = i + PF; j < i + PF + HB; ++j) if (j < 43) hf[j] = hld(j);
      }
      if (i + DA == 16) ev_post(EV_M);
      __builtin_amdgcn_sched_barrier(0);
    }
    const u32 pw = L_P + 2048 + kh * 17408 + (32 * tt + n) * 272 + hh * 16;
#pragma unroll
    for (int i = 0; i < 4; ++i) {
      sts128(pw + i * 32, __builtin_bit_cast(u4, f4{d0[4 * i], d0[4 * i + 1], d0[4 * i + 2], d0[4 * i + 3]}));
      sts128(pw + 128 + i * 32, __builtin_bit_cast(u4, f4{d1[4 * i], d1[4 * i + 1], d1[4 * i + 2], d1[4 * i + 3]}));
    }
    bar();
  } else {
    const int hw = w4, ht = hw * 64 + l;
    __builtin_amdgcn_s_setprio(2);
    u32 ti = 0;
    u64 e0 = gld8_sc1(0, ws);
    const int ci = hw >> 1, ii = ht & 127, grp = l >> 4, c8 = l & 7, rl = l >> 3;
    const int row0 = 64 * rb + 16 * hw;
    const u32 stg = L_HC + ci * 16896 + 64 + (hw & 1) * 2048;
#pragma unroll
    for (int j = 0; j < 3; ++j) {
      const int r = row0 - 8 + 8 * j;
      dma16(rl * 1024 + c8 * 16, kk + (r < 0 ? 0 : r) * 1024 + cg * 128, stg + j * 4224);
    }
    dma16((l & 31) * 16, shk + cg * 512, stg + 3 * 4224);
    u4 vr[2], hk[3];
#pragma unroll
    for (int j = 0; j < 2; ++j) vr[j] = gld((2 * rl + j) * 1024 + c8 * 16, vv + row0 * 1024 + cg * 128);
    if (hw == 0) dma16((l & 7) * 16, b2 + cg * 128, L_B);
    if (hw == 1) dma16(((l + 8 * rot1) & 63) * 16, gw, L_GV);
    const u32 dd = *(const AS4 u32*)(size_t)(ssd + 4 * b);
    if (ht < 16) *(AS3 u32*)(size_t)(L_EV + ht * 4) = 0;
    vmwait<0>();
    TIE("+v"(e0), "+v"(vr[0]), "+v"(vr[1]));
    const u64 tag = e0 * 6364136223846793005ull + 1442695040888963407ull;
    if (row0 == 0 && l < 24) sts128(stg + 640 + l * 16, u4{0, 0, 0, 0});
    u4 kr[5], sk[4], qr[2], xv[2], gv[2];
#pragma unroll
    for (int j = 0; j < 5; ++j) kr[j] = lds128(stg + ((5 + 2 * rl + j) >> 3) * 4224 + ((5 + 2 * rl + j) & 7) * 128 + c8 * 16);
#pragma unroll
    for (int j = 0; j < 4; ++j) sk[j] = lds128(stg + 3 * 4224 + c8 * 64 + j * 16);
    float gacc = 0.f;
    const u32 gx = ((rb & 1) * 64 + l) * 128, gs = (l >> 1) & 7;
    auto gate_ld = [&](int c) {
#pragma unroll
      for (int e = 0; e < 2; ++e) {
        xv[e] = lds128((c & 3) * SLOT + gx + (((2 * hw + e) ^ gs) << 4));
        gv[e] = lds128(L_GV + c * 128 + hw * 32 + e * 16);
      }
      __builtin_amdgcn_sched_barrier(0);
    };
    auto gate = [&]() {
#pragma unroll
      for (int e = 0; e < 2; ++e)
#pragma unroll
        for (int i = 0; i < 4; ++i) {
          gacc = dot2(gv[e][i], xv[e][i], gacc);
        }
      TIE("+v"(gacc));
    };
    auto q_ld = [&]() {
#pragma unroll
      for (int e = 0; e < 2; ++e)
        qr[e] = lds128(((rb & 1) * 64 + 16 * hw + (l & 15)) * 128 + (((grp + 4 * e) ^ ((l & 15) >> 1)) << 4));
    };
#pragma unroll
    for (int e = 0; e < 8; ++e) {
      const u32 sel = (e & 1) ? 0x07060302u : 0x05040100u;
      const int m = e >> 1;
      const u32 sa = sk[m][(e & 1) * 2], sb = sk[m][(e & 1) * 2 + 1];
      float r0 = dot2(__builtin_amdgcn_perm(kr[0][m], kr[1][m], sel), sb, 0.f);
      float r1 = dot2(__builtin_amdgcn_perm(kr[1][m], kr[2][m], sel), sb, 0.f);
      r0 = dot2(__builtin_amdgcn_perm(kr[2][m], kr[3][m], sel), sa, r0);
      r1 = dot2(__builtin_amdgcn_perm(kr[3][m], kr[4][m], sel), sa, r1);
      r0 *= (e & 1) ? bhi(vr[0][m]) : blo(vr[0][m]);
      r1 *= (e & 1) ? bhi(vr[1][m]) : blo(vr[1][m]);
      *(AS3 u32*)(size_t)(L_U + c8 * 1056 + e * 128 + (8 * hw + rl) * 4) = pk(r0, r1);
    }
    const u32 cp = L_HC + ci * 16896 + 64 + ii * 32;
    auto copies = [&](int s) {
      if (s == 0) {
#pragma unroll
        for (int j = 0; j < 3; ++j) hk[j] = lds128(cp - 16 + 16 * j);
      }
      u32 W[13];
#pragma unroll
      for (int j = 0; j < 12; ++j) W[j] = hk[j >> 2][j & 3];
      W[12] = 0;
      auto A = [&](int j) { return __builtin_amdgcn_alignbit(W[j + 1], W[j], 16); };
      if (s == 0) {
        sts128(cp + 4224, u4{W[3], W[4], W[5], W[6]});        sts128(cp + 4240, u4{W[7], W[8], W[9], W[10]});
        if (ii == 127) sts128(cp + 4224 + 32, u4{W[11], 0, 0, 0});
        if (hw < 2) sts128(L_U + (hw * 4 + grp) * 1280 + (l & 15) * 16, u4{0, 0, 0, 0});
      } else if (s == 1) {
        sts128(cp + 8448, u4{A(1), A(2), A(3), A(4)});        sts128(cp + 8464, u4{A(5), A(6), A(7), A(8)});
        if (ii == 127) sts128(cp + 8448 + 32, u4{A(9), A(10), A(11), 0});
      } else {
        sts128(cp + 12672, u4{A(0), A(1), A(2), A(3)});       sts128(cp + 12688, u4{A(4), A(5), A(6), A(7)});
        if (ii == 127) sts128(cp + 12672 + 32, u4{A(8), A(9), A(10), A(11)});
      }
    };
    if (ii < 16 || ii == 127) sts128(ii < 16 ? L_HC + ci * 16896 + (ii >> 2) * 4224 + (ii & 3) * 16 : cp + 32, u4{0, 0, 0, 0});
    bar();
#pragma unroll
    for (int e = 0; e < 2; ++e) {
      const int ch = 16 * hw + 8 * e + rl;
      u4 v = lds128(L_U + (ch >> 3) * 1056 + (ch & 7) * 128 + c8 * 16);
      __builtin_amdgcn_raw_buffer_store_b128(v, rws, WS_KVT + (64 * cg + ch) * 4096 + rb * 128 + c8 * 16, 0, 16);
    }
    {
#pragma unroll
      for (int e = 0; e < 2; ++e)
        __builtin_amdgcn_raw_buffer_store_b128(u4{~0u, ~0u, ~0u, ~0u}, rws, WS_YT + b * 8192 + (2 * hw + e) * 1024 + l * 16, 0, 16);
    }
    gate_ld(0);
    q_ld();
#pragma unroll
    for (int j = 0; j < 2; ++j) dma16(l * 16, ssk + (2 * b + ci) * 4096 + (hw & 1) * 2048 + j * 1024, stg + j * 1024);
    gate();
    auto feed = [&](int c) {
      gate_ld(c);
#pragma unroll
      for (int j = 0; j < 7; ++j) { dma_piece(c + 3, j); if (j == GP) gate(); }
    };
    u32 t0 = 0, t1 = 0;
    constexpr int NT = (PW2 ? 1 : 0), TP = PW2 ? PW2 : 5;
    auto warm = [&](int c) {
      if (c != TP) return;
      if (PW2) {
        const int i = min((bi >> 3) * 172 + ht, 5503);
        t0 = touch((i >> 2) * 1024 + (i & 3) * 128, w2 + (cg >> 2) * 512);
      }
    };
    bar();
    feed(1); warm(1);
    bar();
    feed(2); warm(2);
    bar();
    feed(3); warm(3);
    vmwait<14 + (TP <= 3 ? NT : 0)>();
    bar();
    if (qh) q_ld();
    feed(4); warm(4);
    vmwait<14 + (TP >= 2 && TP <= 4 ? NT : 0)>();
    bar();
    auto w2s = [&](int sl) {
      if (sl < HS)
#pragma unroll
        for (int j = 7 * sl; j < 7 * sl + 7; ++j) dma16(w2o, w2p(4 * j + hw), (4 * j + hw) * 1024);
    };
    gate_ld(5); warm(5); w2s(0); copies(0); gate();
    vmwait<7 + (TP >= 3 && TP <= 5 ? NT : 0) + (HS > 0 ? 7 : 0)>();
    bar();
    gate_ld(6); warm(6); w2s(1); copies(1); gate();
    vmwait<(TP >= 4 ? NT : 0) + (HS > 0 ? 7 : 0) + (HS > 1 ? 7 : 0)>();
    bar();
    u64 fv = gld8_sc1(fkv(cg, l & 31), ws);
    gate_ld(7); w2s(2); copies(2); gate();
    *(AS3 float*)(size_t)(L_GW + ht * 4) = gacc;
    bar();
    vmwait<(HS > 2 ? 7 : 0)>();
    TIE("+v"(fv), "+v"(t0), "+v"(t1), "+v"(ti));
    poll_from(fv, ws + fkv(cg, l & 31), tag);
    u4 wr[15];
    auto wr_ld = [&](int k) { wr[k] = gld(w2o, w2p(k < 10 ? 112 + 10 * hw + k : 142 + 5 * hw + k)); };
    {
      u4 uu[2];
#pragma unroll
      for (int e = 0; e < 2; ++e) uu[e] = gld_sc1(((hw & 1) * 128 + e * 64 + l) * 16, ws + WS_KVT + (2 * b + ci) * 4096);
#pragma unroll
      for (int k = 0; k < (WRP ? 0 : 15); ++k) wr_ld(k);
      vmwait<(WRP ? 0 : 15)>();
      TIE("+v"(uu[0]), "+v"(uu[1]));
      if (HS) ev_post(EV_W2A);
#pragma unroll
      for (int e = 0; e < 2; ++e) {
        const int p = (hw & 1) * 128 + e * 64 + l;
        sts128(L_U + ci * 5120 + (p & 3) * 1280 + ((p >> 2) + 16) * 16, uu[e]);
      }
    }
    ev_post(EV_U); ev_wait(EV_U, 4);
    __builtin_amdgcn_s_setprio(CPRIO);
    {
      const int hf = hw & 1, G = l >> 4, tq = (l >> 2) & 3, tp = l & 3;
      const u32 ab = L_HC + ci * 16896 + (tq & 1) * 8448 + (tq >> 1) * 4224 + (8 * tp - 8 * G + 28 + (tq & 1) * 4 + 256 * hf) * 2;
      const u32 ub = L_U + ci * 5120 + G * 1280 + ((l & 15) + 9 - 8 * hf) * 16;
      f4 y[4][2];
#pragma unroll
      for (int j = 0; j < 4; ++j) { y[j][0] = f4{0, 0, 0, 0}; y[j][1] = f4{0, 0, 0, 0}; }
      u4 Bn[4]; bf4 An[12];
      auto rd = [&](int st, int m) {
        if (m < 3) An[m] = ldtr(ab + 64 * st + 8 * m);
        else if (m < 7) Bn[m - 3] = lds128(ub + (16 * (m - 3) + 7 - st) * 16);
        else An[m - 4] = ldtr(ab + 1024 * ((m - 4) / 3) + 64 * st + 8 * ((m - 4) % 3));
      };
#pragma unroll
      for (int m = 0; m < 16; ++m) rd(0, m);
      ev_wait(EV_G, GN);
#pragma unroll
      for (int st = 0; st < 8; ++st) {
        u4 B[4]; bf4 A[12];
#pragma unroll
        for (int j = 0; j < 4; ++j) B[j] = Bn[j];
#pragma unroll
        for (int j = 0; j < 12; ++j) A[j] = An[j];
        int m = 0;
#pragma unroll
        for (int i = 0; i < 4; ++i) {
#pragma unroll
          for (int j = 0; j + i < 4; ++j)
#pragma unroll
            for (int r = 0; r < 2; ++r) {
              __builtin_amdgcn_sched_barrier(0);
              y[j + i][r] = __builtin_amdgcn_mfma_f32_16x16x32_bf16(cat(A[3 * i + 1 + r], A[3 * i + r]),
                                                                   __builtin_bit_cast(bf8, B[j]), y[j + i][r], 0, 0, 0);
              __builtin_amdgcn_sched_barrier(0);
              if (st < 7 && m < 16) rd(st + 1, m);
              if (WRP && st >= WRP && st < WRP + 3 && m % 4 == 3) wr_ld(5 * (st - WRP) + m / 4);
              ++m;
            }
        }
      }
      __builtin_amdgcn_s_setprio(2);
      const u32 xa = L_HC + ci * 16896 + G * 4224 + 128 + (l & 15) * 16;
      f4 own[4];
#pragma unroll
      for (int m = 0; m < 4; ++m) {
        own[m] = hf ? y[2 + (m >> 1)][m & 1] : y[m >> 1][m & 1];
        sts128(xa + 1024 * m + 512 * hf, __builtin_bit_cast(u4, hf ? y[m >> 1][m & 1] : y[2 + (m >> 1)][m & 1]));
      }
      ev_post(EV_PA + ci);
      ev_wait(EV_PA + ci, 2);
      u4 pr[4], u[2];
#pragma unroll
      for (int m = 0; m < 4; ++m) pr[m] = lds128(xa + 1024 * m + 512 * (1 - hf));
#pragma unroll
      for (int e = 0; e < 2; ++e) u[e] = lds128(L_U + ci * 5120 + G * 1280 + (16 * (2 * hf + e) + (l & 15) + 16) * 16);
      ev_post(EV_CD);
      const float D = ci ? bhi(dd) : blo(dd);
      const int src = ((l >> 2) + 16 * (l & 3)) * 4;
#pragma unroll
      for (int e = 0; e < 2; ++e) {
        u4 zo;
#pragma unroll
        for (int r = 0; r < 2; ++r) {
          const f4 s = own[2 * e + r] + __builtin_bit_cast(f4, pr[2 * e + r]);
          zo[2 * r] = __builtin_amdgcn_ds_bpermute(src, pk(s[0] + D * blo(u[e][2 * r]), s[1] + D * bhi(u[e][2 * r])));
          zo[2 * r + 1] = __builtin_amdgcn_ds_bpermute(src, pk(s[2] + D * blo(u[e][2 * r + 1]), s[3] + D * bhi(u[e][2 * r + 1])));
        }
        {
#pragma unroll
          for (int i = 0; i < 4; ++i) zo[i] = min(zo[i], 0xfffffffeu);
        }
        __builtin_amdgcn_raw_buffer_store_b128(zo, rws, WS_YT + (2 * b + ci) * 4096 + (2 * hf + e) * 1024 + l * 16, 0, 16);
      }
    }
    vmwait<2>();
    TIE("+v"(wr[0]), "+v"(wr[1]), "+v"(wr[2]), "+v"(wr[3]), "+v"(wr[4]), "+v"(wr[5]), "+v"(wr[6]), "+v"(wr[7]));
    TIE("+v"(wr[8]), "+v"(wr[9]), "+v"(wr[10]), "+v"(wr[11]), "+v"(wr[12]), "+v"(wr[13]), "+v"(wr[14]));
    ev_wait(EV_CD, 4);
#pragma unroll
    for (int j = 0; j < 10; ++j) sts128((112 + 10 * hw + j) * 1024 + l * 16, wr[j]);
    ev_post(EV_W2B);
    const u32 zs = (64 * cg + 16 * hw + (l >> 3)) * 4096 + rb * 128 + (l & 7) * 16;
    u4 z0, z1;
    ev_wait(EV_M, 4);
#pragma unroll
    for (int j = 0; j < 5; ++j) sts128((5 * hw + j) * 1024 + l * 16, wr[10 + j]);
    ev_post(EV_W2C);
    {
      z0 = gld_sc1(zs, ws + WS_YT); z1 = gld_sc1(zs, ws + WS_YT + 32768);
      vmwait<0>();
      TIE("+v"(z0), "+v"(z1));
#pragma unroll 1
      for (int it = 0; it < SPIN_MAX; ++it) {
        const u32 mx = max(max(max(z0[0], z0[1]), max(z0[2], z0[3])), max(max(z1[0], z1[1]), max(z1[2], z1[3])));
        if (__builtin_amdgcn_ballot_w64(mx == ~0u) == 0) break;
        __builtin_amdgcn_s_sleep(ZSL);
        z0 = gld_sc1(zs, ws + WS_YT); z1 = gld_sc1(zs, ws + WS_YT + 32768);
        vmwait<0>();
        TIE("+v"(z0), "+v"(z1));
      }
      sts128(L_Z + 2048 + (16 * hw + (l >> 3)) * 144 + (l & 7) * 16, z0);
      sts128(L_Z + 2048 + (16 * hw + 8 + (l >> 3)) * 144 + (l & 7) * 16, z1);
    }
    ev_post(EV_Z); ev_wait(EV_Z, 4);
    float gl = 0.f;
#pragma unroll
    for (int j = 0; j < 4; ++j) gl += *(AS3 float*)(size_t)(L_GW + (64 * j + 16 * hw + (l & 15)) * 4);
    const float gt = __builtin_amdgcn_rcpf(1.f + __builtin_amdgcn_exp2f(-1.4426950408889634f * gl));
    float ap[2][8];
#pragma unroll
    for (int e = 0; e < 2; ++e) {
      const u32 a = L_Z + 2048 + (8 * (grp + 4 * e) + ((l >> 2) & 3)) * 144 + (16 * hw + 4 * (l & 3)) * 2;
      u4 z = __builtin_bit_cast(u4, cat(ldtr(a), ldtr(a + 4 * 144)));
      const u4 br = lds128(L_B + (grp + 4 * e) * 16);
#pragma unroll
      for (int i = 0; i < 4; ++i) {
        ap[e][2 * i] = gt * blo(qr[e][i]) * blo(z[i]) + (1.f - gt) * blo(br[i]);
        ap[e][2 * i + 1] = gt * bhi(qr[e][i]) * bhi(z[i]) + (1.f - gt) * bhi(br[i]);
      }
    }
    bar();
#pragma unroll
    for (int e = 0; e < 2; ++e) {
      const u32 a = L_P + 2048 + (16 * hw + (l & 15)) * 272 + (grp + 4 * e) * 32;
      u4 o;
#pragma unroll
      for (int r = 0; r < 2; ++r) {
        f4 s = __builtin_bit_cast(f4, lds128(a + r * 16)) + __builtin_bit_cast(f4, lds128(a + 17408 + r * 16));
        o[2 * r] = pk(ap[e][4 * r] + (1.f - gt) * s[0], ap[e][4 * r + 1] + (1.f - gt) * s[1]);
        o[2 * r + 1] = pk(ap[e][4 * r + 2] + (1.f - gt) * s[2], ap[e][4 * r + 3] + (1.f - gt) * s[3]);
      }
      __builtin_amdgcn_raw_buffer_store_b128(o, rout, (row0 + (l & 15)) * 1024 + cg * 128 + (grp + 4 * e) * 16, 0, OAUX);
    }
  }
}

extern "C" void kernel_launch(void* const* d_in, const int* in_sizes, int n_in, void* d_out, int out_size, void* d_ws,
                              size_t ws_size, hipStream_t stream) {
  (void)in_sizes; (void)n_in; (void)out_size; (void)ws_size;
  (void)hipFuncSetAttribute((const void*)fused_kernel, hipFuncAttributeMaxDynamicSharedMemorySize, LDS_BYTES);
  Args a = {(const char*)d_in[0], (const char*)d_in[1], (const char*)d_in[2], (const char*)d_in[3], (const char*)d_in[4],
            (const char*)d_in[5], (const char*)d_in[7], (const char*)d_in[8], (const char*)d_in[9], (const char*)d_in[10],
            (const char*)d_in[11], (char*)d_out, (char*)d_ws};
  fused_kernel<<<256, 512, LDS_BYTES, stream>>>(a);
}
```
